```python
import torch

def _softmax(x, axis=-1):
    return torch.softmax(x, dim=axis)

def _gelu(x):
    return torch.nn.functional.gelu(x, approximate='tanh')

def _silu(x):
    return torch.nn.functional.silu(x)

def _sigmoid(x):
    return torch.sigmoid(x)

def _layernorm(x, axis=-1, eps=1e-05):
    m = x.mean(dim=axis, keepdim=True)
    v = x.var(dim=axis, keepdim=True, unbiased=False)
    return (x - m) / torch.sqrt(v + eps)

def _scatter_add(tgt, idx, src, axis=0):
    return tgt.clone().index_add_(axis, idx, src)

def _segment_sum(src, seg, n):
    out = torch.zeros((n,) + src.shape[1:], dtype=src.dtype)
    return out.index_add_(0, seg, src)

def _pad(x, pw):
    flat = tuple((v for pair in reversed(pw) for v in pair))
    return torch.nn.functional.pad(x, flat)
kernel_name = 'adaLN_cond__mha_block_cached__geglu_ffn__decode'

def setup_inputs():

    _w = lambda a: (lambda t: t.to(torch.bfloat16) if t.is_floating_point() else t)(torch.as_tensor(a))

    def _A_setup():
        B, Sq, D = (4, 1, 512)
        H, Sk, Dh = (32, 8192, 128)
        return {'x': _w((lambda *s: torch.randn(*s, dtype=torch.bfloat16) * 0.05)(B, Sq, D)), 'k_cache': _w((lambda *s: torch.randn(*s, dtype=torch.bfloat16) * 3.2)(B, H, Sk, Dh)), 'v_cache': _w((lambda *s: torch.randn(*s, dtype=torch.bfloat16) * 0.4)(B, H, Sk, Dh)), 'wq': _w((lambda *s: torch.randn(*s, dtype=torch.bfloat16) * 3.2)(D, H * Dh) * D ** (-0.5)), 'wo': _w((lambda *s: torch.randn(*s, dtype=torch.bfloat16) * 0.4)(H * Dh, D) * (H * Dh) ** (-0.5))}
    _A_in = _A_setup()

    def _B_setup():
        B, S, D, F = (4, 1, 512, 1792)
        return {'x': _w((lambda *s: torch.randn(*s, dtype=torch.bfloat16) * 0.05)(B, S, D)), 'w_gate': _w((lambda *s: torch.randn(*s, dtype=torch.bfloat16) * 0.05)(D, F)), 'w_up': _w((lambda *s: torch.randn(*s, dtype=torch.bfloat16) * 0.05)(D, F)), 'w_down': _w((lambda *s: torch.randn(*s, dtype=torch.bfloat16) * 0.05)(F, D))}
    _B_in = _B_setup()
    return {'A_x': _A_in['x'], 'A_k_cache': _A_in['k_cache'], 'A_v_cache': _A_in['v_cache'], 'A_wq': _A_in['wq'], 'A_wo': _A_in['wo'], 'B_x': _B_in['x'], 'B_w_gate': _B_in['w_gate'], 'B_w_up': _B_in['w_up'], 'B_w_down': _B_in['w_down'], 'c': _w((lambda *s: torch.randn(*s, dtype=torch.bfloat16) * 0.1)(4, 512) * 8.0), 'w_mod': _w((lambda *s: torch.randn(*s, dtype=torch.bfloat16) * 0.1)(512, 6 * 512) * 512 ** (-0.5) * 8.0)}

def reference(A_x, A_k_cache, A_v_cache, A_wq, A_wo, B_x, B_w_gate, B_w_up, B_w_down, c, w_mod):

    def _A_ref(x, k_cache, v_cache, wq, wo):
        B, Sq, D = x.shape
        H, Dh = (32, 128)
        q = (lambda x, *d: x.permute(*d))((x @ wq).reshape(B, Sq, H, Dh), 0, 2, 1, 3)
        scores = torch.einsum('bhqd,bhkd->bhqk', q, k_cache) * Dh ** (-0.5)
        p = _softmax(scores, axis=-1)
        o = torch.einsum('bhqk,bhkd->bhqd', p, v_cache)
        return (lambda x, *d: x.permute(*d))(o, 0, 2, 1, 3).reshape(B, Sq, H * Dh) @ wo

    def _B_ref(x, w_gate, w_up, w_down):
        return _gelu(x @ w_gate) * (x @ w_up) @ w_down
    x = A_x
    mod = (_silu(c) @ w_mod).reshape(c.shape[0], 6, 1, -1)
    sa, ca, ga, sb, cb, gb = (mod[:, 0].clone(), mod[:, 1].clone(), mod[:, 2].clone(), mod[:, 3].clone(), mod[:, 4].clone(), mod[:, 5].clone())
    h = _layernorm(x, axis=-1) * (1 + ca) + sa
    x = x + ga * _A_ref(h, A_k_cache, A_v_cache, A_wq, A_wo)
    h = _layernorm(x, axis=-1) * (1 + cb) + sb
    x = x + gb * _B_ref(h, B_w_gate, B_w_up, B_w_down)
    return x
```

```cpp
#include <hip/hip_runtime.h>
#include <hip/hip_bf16.h>
#include <cstdint>

#define MIMIC 1

typedef unsigned short bf16_t;
using u32x4  = __attribute__((ext_vector_type(4))) unsigned;
using bf16x2 = __attribute__((ext_vector_type(2))) __bf16;

constexpr int B = 4, D = 512, H = 32, SK = 8192, DH = 128, F = 1792;
constexpr int NSPLIT = 8;

__device__ __forceinline__ float bf2f(bf16_t v) { return __builtin_bit_cast(float, (unsigned)v << 16); }
__device__ __forceinline__ bf16_t f2bf(float f) { return __builtin_bit_cast(bf16_t, (__bf16)f); }
__device__ __forceinline__ float rb(float f) {
  return bf2f(f2bf(f));
}
__device__ __forceinline__ float dot2(unsigned k, unsigned q, float acc) {
  return __builtin_amdgcn_fdot2_f32_bf16(__builtin_bit_cast(bf16x2, k), __builtin_bit_cast(bf16x2, q), acc, false);
}
template <int CTRL> __device__ __forceinline__ float dpp(float x) {
  return __builtin_bit_cast(float, __builtin_amdgcn_mov_dpp(__builtin_bit_cast(int, x), CTRL, 0xf, 0xf, true));
}
__device__ __forceinline__ float block_sum(float v, float* red) {
  for (int m = 32; m >= 1; m >>= 1) v += __shfl_xor(v, m);
  const int w = threadIdx.x >> 6, nw = blockDim.x >> 6;
  __syncthreads();
  if ((threadIdx.x & 63) == 0) red[w] = v;
  __syncthreads();
  float s = 0.f;
  for (int i = 0; i < nw; ++i) s += red[i];
  return s;
}

template <int K, int N>
__device__ __forceinline__ void skinny(const float* a_lds  , const bf16_t* W, int n0, float* red  , float* outv  ) {
  const int tid = threadIdx.x, cg = tid & 7, kg = tid >> 3;
  float acc[4][8];
#pragma unroll
  for (int b = 0; b < 4; ++b)
#pragma unroll
    for (int j = 0; j < 8; ++j) acc[b][j] = 0.f;
  for (int k = kg; k < K; k += 32) {
    u32x4 w = *reinterpret_cast<const u32x4*>(W + (size_t)k * N + n0 + cg * 8);
    float wf[8];
#pragma unroll
    for (int j = 0; j < 4; ++j) { wf[2 * j] = __builtin_bit_cast(float, w[j] << 16); wf[2 * j + 1] = __builtin_bit_cast(float, w[j] & 0xffff0000u); }
#pragma unroll
    for (int b = 0; b < 4; ++b) { const float av = a_lds[b * K + k];
#pragma unroll
      for (int j = 0; j < 8; ++j) acc[b][j] = fmaf(av, wf[j], acc[b][j]); }
  }
  __syncthreads();
#pragma unroll
  for (int b = 0; b < 4; ++b)
#pragma unroll
    for (int j = 0; j < 8; ++j) red[(kg * 4 + b) * 64 + cg * 8 + j] = acc[b][j];
  __syncthreads();
  float s = 0.f;
  for (int g = 0; g < 32; ++g) s += red[(g * 4 + (tid >> 6)) * 64 + (tid & 63)];
  *outv = s;
}

__global__ void __launch_bounds__(256) k_mod(const bf16_t* c, const bf16_t* wmod, float* mod) {
  __shared__ float a[4 * D]; __shared__ float red[32 * 4 * 64];
  for (int i = threadIdx.x; i < 4 * D; i += 256) { float v = bf2f(c[i]); a[i] = rb(v / (1.f + expf(-v))); }
  __syncthreads();
  float o; skinny<D, 6 * D>(a, wmod, blockIdx.x * 64, red, &o);
  mod[(threadIdx.x >> 6) * 6 * D + blockIdx.x * 64 + (threadIdx.x & 63)] = rb(o);
}

__device__ __forceinline__ void ln_mod(const float* xs, const float* mod, int shift_idx, int scale_idx, float* a) {
  const int b = threadIdx.x >> 6, lane = threadIdx.x & 63;
  float s = 0.f;
  for (int i = lane; i < D; i += 64) s += xs[b * D + i];
  for (int m = 32; m >= 1; m >>= 1) s += __shfl_xor(s, m);
  const float mean = s * (1.f / D);
  float v = 0.f;
  for (int i = lane; i < D; i += 64) { float d = xs[b * D + i] - mean; v += d * d; }
  for (int m = 32; m >= 1; m >>= 1) v += __shfl_xor(v, m);
  const float var = rb(v * (1.f / D));
  const float mr = rb(mean);
  const float sd = rb(sqrtf(rb(var + 1e-5f)));
  for (int i = lane; i < D; i += 64) {
    float n = rb(rb(xs[b * D + i] - mr) / sd);
    float sc = rb(1.f + mod[b * 6 * D + scale_idx * D + i]);
    a[b * D + i] = rb(rb(n * sc) + mod[b * 6 * D + shift_idx * D + i]);
  }
}

__global__ void __launch_bounds__(256) k_q(const bf16_t* x, const float* mod, const bf16_t* wq, float* q) {
  __shared__ float xs[4 * D]; __shared__ float a[4 * D]; __shared__ float red[32 * 4 * 64];
  for (int i = threadIdx.x; i < 4 * D; i += 256) xs[i] = bf2f(x[i]);
  __syncthreads();
  ln_mod(xs, mod, 0, 1, a);
  __syncthreads();
  float o; skinny<D, H * DH>(a, wq, blockIdx.x * 64, red, &o);
  q[(threadIdx.x >> 6) * H * DH + blockIdx.x * 64 + (threadIdx.x & 63)] = rb(o);
}

constexpr int XOR1 = 0xB1, XOR2 = 0x4E, XOR7 = 0x141, XOR8 = 0x128;
__global__ void __launch_bounds__(256) k_attn(const float* q, const bf16_t* kc, const bf16_t* vc, float* part  ) {
  constexpr int KEYS = SK / NSPLIT;
  __shared__ float sc[KEYS]; __shared__ float red[16]; __shared__ float ored[16 * DH];
  const int s = blockIdx.x, h = blockIdx.y, b = blockIdx.z, tid = threadIdx.x, lane = tid & 63, wave = tid >> 6;
  const int r = lane >> 4, c = lane & 15;
  const size_t base = ((size_t)(b * H + h) * SK + (size_t)s * KEYS) * DH;
  float qf[8];
#pragma unroll
  for (int j = 0; j < 8; ++j) qf[j] = q[(b * H + h) * DH + c * 8 + j];
  for (int i = 0; i < KEYS / 16; i += 4) {
    u32x4 kv[4];
#pragma unroll
    for (int t = 0; t < 4; ++t) kv[t] = __builtin_nontemporal_load(reinterpret_cast<const u32x4*>(kc + base + (size_t)((i + t) * 16 + wave * 4 + r) * DH + c * 8));
#pragma unroll
    for (int t = 0; t < 4; ++t) {
      float d = 0.f;
#pragma unroll
      for (int j = 0; j < 4; ++j) { d = fmaf(__builtin_bit_cast(float, kv[t][j] << 16), qf[2 * j], d); d = fmaf(__builtin_bit_cast(float, kv[t][j] & 0xffff0000u), qf[2 * j + 1], d); }
      d += dpp<XOR8>(d); d += dpp<XOR7>(d); d += dpp<XOR2>(d); d += dpp<XOR1>(d);
      if (c == 0) sc[(i + t) * 16 + wave * 4 + r] = rb(rb(d) * 0.08838834764831845f);
    }
  }
  __syncthreads();
  float m = -INFINITY;
  for (int i = tid; i < KEYS; i += 256) m = fmaxf(m, sc[i]);
  for (int o = 32; o >= 1; o >>= 1) m = fmaxf(m, __shfl_xor(m, o));
  if (lane == 0) red[wave] = m;
  __syncthreads();
  m = fmaxf(fmaxf(red[0], red[1]), fmaxf(red[2], red[3]));
  float l = 0.f;
  for (int i = tid; i < KEYS; i += 256) { float p = __expf(sc[i] - m); sc[i] = p; l += p; }
  l = block_sum(l, red + 8);
  __syncthreads();
  float acc[8];
#pragma unroll
  for (int j = 0; j < 8; ++j) acc[j] = 0.f;
  for (int i = 0; i < KEYS / 16; i += 4) {
    u32x4 vv[4];
#pragma unroll
    for (int t = 0; t < 4; ++t) vv[t] = __builtin_nontemporal_load(reinterpret_cast<const u32x4*>(vc + base + (size_t)((i + t) * 16 + wave * 4 + r) * DH + c * 8));
#pragma unroll
    for (int t = 0; t < 4; ++t) { const float p = sc[(i + t) * 16 + wave * 4 + r];
#pragma unroll
      for (int j = 0; j < 4; ++j) { acc[2 * j] = fmaf(p, __builtin_bit_cast(float, vv[t][j] << 16), acc[2 * j]); acc[2 * j + 1] = fmaf(p, __builtin_bit_cast(float, vv[t][j] & 0xffff0000u), acc[2 * j + 1]); } }
  }
#pragma unroll
  for (int j = 0; j < 8; ++j) ored[(wave * 4 + r) * DH + c * 8 + j] = acc[j];
  __syncthreads();
  float* po = part + ((size_t)(b * H + h) * NSPLIT + s) * 132;
  if (tid < DH) { float o = 0.f; for (int g = 0; g < 16; ++g) o += ored[g * DH + tid]; po[4 + tid] = o; }
  if (tid == 0) { po[0] = m; po[1] = l; }
}

__global__ void __launch_bounds__(256) k_wo(const float* part, const bf16_t* wo, const bf16_t* x, const float* mod, float* x1) {
  extern __shared__ float dyn[];
  float* a = dyn; float* red = dyn + 4 * H * DH;
  for (int i = threadIdx.x; i < 4 * H * DH; i += 256) {
    const int bh = i >> 7, d = i & 127;
    const float* p = part + (size_t)bh * NSPLIT * 132;
    float M = -INFINITY;
    for (int s = 0; s < NSPLIT; ++s) M = fmaxf(M, p[s * 132]);
    float L = 0.f, o = 0.f;
    for (int s = 0; s < NSPLIT; ++s) { float w = __expf(p[s * 132] - M); L += w * p[s * 132 + 1]; o += w * p[s * 132 + 4 + d]; }
    a[i] = rb(o / L);
  }
  __syncthreads();
  float o; skinny<H * DH, D>(a, wo, blockIdx.x * 64, red, &o);
  const int b = threadIdx.x >> 6, n = blockIdx.x * 64 + (threadIdx.x & 63);
  x1[b * D + n] = rb(bf2f(x[b * D + n]) + rb(mod[b * 6 * D + 2 * D + n] * rb(o)));
}

__global__ void __launch_bounds__(256) k_ffn1(const float* x1, const float* mod, const bf16_t* wg, const bf16_t* wu, float* act) {
  __shared__ float xs[4 * D]; __shared__ float a[4 * D]; __shared__ float red[32 * 4 * 64];
  for (int i = threadIdx.x; i < 4 * D; i += 256) xs[i] = x1[i];
  __syncthreads();
  ln_mod(xs, mod, 3, 4, a);
  __syncthreads();
  float g, u; skinny<D, F>(a, wg, blockIdx.x * 64, red, &g); skinny<D, F>(a, wu, blockIdx.x * 64, red, &u);
  g = rb(g); u = rb(u);
  const float inner = 0.7978845608028654f * (g + 0.044715f * g * g * g);
  const float ge = rb(0.5f * g * (1.f + tanhf(inner)));
  act[(threadIdx.x >> 6) * F + blockIdx.x * 64 + (threadIdx.x & 63)] = rb(ge * u);
}

__global__ void __launch_bounds__(256) k_ffn2(const float* act, const bf16_t* wd, const float* x1, const float* mod, bf16_t* out) {
  __shared__ float a[4 * F]; __shared__ float red[32 * 4 * 64];
  for (int i = threadIdx.x; i < 4 * F; i += 256) a[i] = act[i];
  __syncthreads();
  float o; skinny<F, D>(a, wd, blockIdx.x * 64, red, &o);
  const int b = threadIdx.x >> 6, n = blockIdx.x * 64 + (threadIdx.x & 63);
  out[b * D + n] = f2bf(x1[b * D + n] + rb(mod[b * 6 * D + 5 * D + n] * rb(o)));
}

extern "C" void kernel_launch(void* const* d_in, const int* in_sizes, int n_in, void* d_out, int out_size, void* d_ws, size_t ws_size, hipStream_t stream) {
  const bf16_t* x = (const bf16_t*)d_in[0]; const bf16_t* kc = (const bf16_t*)d_in[1]; const bf16_t* vc = (const bf16_t*)d_in[2];
  const bf16_t* wq = (const bf16_t*)d_in[3]; const bf16_t* wo = (const bf16_t*)d_in[4];
  const bf16_t* wg = (const bf16_t*)d_in[6]; const bf16_t* wu = (const bf16_t*)d_in[7]; const bf16_t* wd = (const bf16_t*)d_in[8];
  const bf16_t* c = (const bf16_t*)d_in[9]; const bf16_t* wmod = (const bf16_t*)d_in[10];
  float* ws = (float*)d_ws;
  float* mod = ws; float* q = mod + 4 * 6 * D; float* x1 = q + 4 * H * DH; float* act = x1 + 4 * D; float* part = act + 4 * F;
  k_mod<<<6 * D / 64, 256, 0, stream>>>(c, wmod, mod);
  k_q<<<H * DH / 64, 256, 0, stream>>>(x, mod, wq, q);
  k_attn<<<dim3(NSPLIT, H, B), 256, 0, stream>>>(q, kc, vc, part);
  static bool attr = false;
  if (!attr) { hipFuncSetAttribute((const void*)k_wo, hipFuncAttributeMaxDynamicSharedMemorySize, (4 * H * DH + 32 * 4 * 64) * 4); attr = true; }
  k_wo<<<D / 64, 256, (4 * H * DH + 32 * 4 * 64) * 4, stream>>>(part, wo, x, mod, x1);
  k_ffn1<<<F / 64, 256, 0, stream>>>(x1, mod, wg, wu, act);
  k_ffn2<<<D / 64, 256, 0, stream>>>(act, wd, x1, mod, (bf16_t*)d_out);
}
```

```cpp
#include <hip/hip_runtime.h>
#include <hip/hip_bf16.h>
#include <cstdint>

typedef unsigned short u16;
typedef unsigned u32;
typedef unsigned long long u64;
using u32x2 = __attribute__((ext_vector_type(2))) unsigned;
using u32x4 = __attribute__((ext_vector_type(4))) unsigned;
using f32x2 = __attribute__((ext_vector_type(2))) float;
using f32x4 = __attribute__((ext_vector_type(4))) float;
using bf16x2 = __attribute__((ext_vector_type(2))) __bf16;
using bf16x8 = __attribute__((ext_vector_type(8))) __bf16;
using s16x4 = __attribute__((ext_vector_type(4))) short;
typedef __amdgpu_buffer_rsrc_t rsrc_t;
#define LAS __attribute__((address_space(3)))
#define GAS __attribute__((address_space(1)))
#define DEV __device__ __forceinline__

#define WIN 20
#define STAMPS 0
#define ZDLY 30
#define ADLY 60
#define HDLY 110
#define PSLP 20
#define QDLY 45
#define NPRE 6
#define SLP_BAND 11
#define SLP_WO 7

constexpr int D = 512, H = 32, SK = 8192, DH = 128, F = 1792;
constexpr int NS = 16, UNIT = 8192, NU = 128;
constexpr u32 SENT = 0xFFFFFFFFu;
constexpr int GCAP = 1 << 21, LCAP = 1 << 23;

constexpr u32 WS_E = 0, WS_FLAG = 4096, WS_HG = 8192, WS_MOD = 32768, WS_ML = 65536, WS_X1 = 131072, WS_QG = 524288,
              WS_Y = 1048576, WS_Z = 1572864, WS_PG = 2097152, WS_END = 2621440;
constexpr int L_SC = NS * UNIT, L_STG = L_SC + 8192, L_Q = L_STG + 16384, L_OP = L_Q + 256, L_AROW = L_OP + 1536,
              L_PART = L_AROW + 2048, L_NV = L_PART + 1024, L_CTL = L_NV + 512, L_ST = L_CTL + 256, L_END = L_ST + 256;
static_assert(L_END <= 163840, "LDS");
enum { C_LANDED = 0, C_ABORT, C_QRDY, C_KCNT, C_PCNT, C_TS, C_INIT, C_SCALE, C_KMAX = 8, C_AFREE = 11, C_L = 12, C_FREE = 32 };

DEV float lo2f(u32 v) { return __builtin_bit_cast(float, v << 16); }
DEV float hi2f(u32 v) { return __builtin_bit_cast(float, v & 0xffff0000u); }
DEV u32 pk2(float a, float b) { bf16x2 r = {(__bf16)a, (__bf16)b}; return __builtin_bit_cast(u32, r); }
DEV u32 bfbits(float a) { return (u32)__builtin_bit_cast(u16, (__bf16)a); }
DEV float rb(float a) { return lo2f(bfbits(a)); }
DEV float dot2(u32 a, u32 b, float c) { return __builtin_amdgcn_fdot2_f32_bf16(__builtin_bit_cast(bf16x2, a), __builtin_bit_cast(bf16x2, b), c, false); }
template <int CTRL> DEV u32 dppu(u32 x) { return (u32)__builtin_amdgcn_mov_dpp((int)x, CTRL, 0xf, 0xf, true); }
template <int CTRL> DEV float dpp(float x) { return __builtin_bit_cast(float, dppu<CTRL>(__builtin_bit_cast(u32, x))); }
constexpr int XOR1 = 0xB1, XOR2 = 0x4E, XOR7 = 0x141, XOR8 = 0x128, ROR4 = 0x124, BCAST = 0x150;
DEV float tsum32(float a, float b) { auto s = __builtin_amdgcn_permlane32_swap(__float_as_uint(a), __float_as_uint(b), false, false); return __uint_as_float(s[0]) + __uint_as_float(s[1]); }
DEV float tsum16(float a, float b) { auto s = __builtin_amdgcn_permlane16_swap(__float_as_uint(a), __float_as_uint(b), false, false); return __uint_as_float(s[0]) + __uint_as_float(s[1]); }
DEV float row_sum(float x) { x += dpp<XOR1>(x); x += dpp<XOR2>(x); x += dpp<XOR7>(x); return x + dpp<XOR8>(x); }
DEV float wave_sum(float x) { x = row_sum(x); x = tsum16(x, x); return tsum32(x, x); }
DEV float wave_max(float x) {
  x = fmaxf(x, dpp<XOR1>(x)); x = fmaxf(x, dpp<XOR2>(x)); x = fmaxf(x, dpp<XOR7>(x)); x = fmaxf(x, dpp<XOR8>(x));
  auto s = __builtin_amdgcn_permlane16_swap(__float_as_uint(x), __float_as_uint(x), false, false); x = fmaxf(__uint_as_float(s[0]), __uint_as_float(s[1]));
  auto t = __builtin_amdgcn_permlane32_swap(__float_as_uint(x), __float_as_uint(x), false, false); return fmaxf(__uint_as_float(t[0]), __uint_as_float(t[1]));
}
DEV float rowsum4(float v0, float v1, float v2, float v3) { return tsum16(tsum32(v0, v2), tsum32(v1, v3)); }
DEV f32x4 mfma(u32x4 a, u32x4 b, f32x4 c) { return __builtin_amdgcn_mfma_f32_16x16x32_bf16(__builtin_bit_cast(bf16x8, a), __builtin_bit_cast(bf16x8, b), c, 0, 0, 0); }
DEV float sigmoidf_(float v) { return __builtin_amdgcn_rcpf(1.f + __builtin_amdgcn_exp2f(-1.4426950408889634f * v)); }
DEV u32 clean(u32 v) { return v < SENT ? v : SENT - 1u; }

#define CBAR() asm volatile("" ::: "memory")
template <int N> DEV void wait_vm() { asm volatile("s_waitcnt vmcnt(%0)" :: "i"(N) : "memory"); }
DEV void wait_lgkm() { asm volatile("s_waitcnt lgkmcnt(0)" ::: "memory"); }
DEV u32x4 ld16(rsrc_t r, u32 off) { return __builtin_bit_cast(u32x4, __builtin_amdgcn_raw_buffer_load_b128(r, off, 0, 16)); }
DEV u32x2 ld8(rsrc_t r, u32 off) { return __builtin_bit_cast(u32x2, __builtin_amdgcn_raw_buffer_load_b64(r, off, 0, 16)); }
DEV u32 ld4(rsrc_t r, u32 off) { return __builtin_amdgcn_raw_buffer_load_b32(r, off, 0, 16); }
DEV void st16(rsrc_t r, u32 off, u32x4 v) { __builtin_amdgcn_raw_buffer_store_b128(v, r, off, 0, 16); }
DEV void st8(rsrc_t r, u32 off, u32x2 v) { __builtin_amdgcn_raw_buffer_store_b64(v, r, off, 0, 16); }
DEV void st4(rsrc_t r, u32 off, u32 v) { __builtin_amdgcn_raw_buffer_store_b32(v, r, off, 0, 16); }
DEV bool ok4(u32x4 v) { return v[0] != SENT && v[1] != SENT && v[2] != SENT && v[3] != SENT; }

template <int OFF> DEV void dma_nt(u32 voff, const void* base) { asm volatile("global_load_lds_dwordx4 %0, %1 offset:%2 nt" :: "v"(voff), "s"(base), "i"(OFF) : "memory"); }
DEV void set_m0(u32 v) { asm volatile("s_mov_b32 m0, %0\n\ts_nop 0" :: "s"(v) : "memory"); }
DEV void dma_at(const void* p, u32 m0v) { asm volatile("s_mov_b32 m0, %1\n\ts_nop 0\n\tglobal_load_lds_dwordx4 %0, off" :: "v"(p), "s"(m0v) : "memory"); }

__global__ void __launch_bounds__(512, 2) adaln_block_kernel(const u16* __restrict__ xin, const u16* __restrict__ kc, const u16* __restrict__ vc,
    const u16* __restrict__ wq, const u16* __restrict__ wo, const u16* __restrict__ wg, const u16* __restrict__ wu, const u16* __restrict__ wd,
    const u16* __restrict__ cin, const u16* __restrict__ wmod, u16* __restrict__ out, unsigned char* ws) {
  __shared__ __attribute__((aligned(16))) unsigned char lds_raw[L_END];
  LAS unsigned char* L = (LAS unsigned char*)lds_raw;
  volatile LAS int* ctl = (volatile LAS int*)(L + L_CTL);
  volatile LAS float* ctf = (volatile LAS float*)(L + L_CTL);
  const u32 lbase = (u32)(size_t)L;
  const int tid = threadIdx.x, lane = tid & 63, wave = __builtin_amdgcn_readfirstlane(tid >> 6);
  const int blk = blockIdx.x, x = blk & 7, g = blk >> 3;
  const int head = (g >> 3) * 8 + x, bat = (g >> 1) & 3, side = g & 1;
#define STAMP(i) do {} while (0)
#define STAMP_OUT() do {} while (0)
  auto lwait = [&](int idx, int want, bool nap) {
    int n = 0;
    while (ctl[idx] < want) {
      if (nap) __builtin_amdgcn_s_sleep(1);
      if ((++n & 255) == 0) { if (ctl[C_ABORT]) break; if (n > LCAP) { ctl[C_ABORT] = 1; break; } }
    }
    CBAR();
  };
  auto larrive = [&](int idx) { wait_lgkm(); if (lane == 0) __hip_atomic_fetch_add((LAS int*)(L + L_CTL) + idx, 1, __ATOMIC_RELAXED, __HIP_MEMORY_SCOPE_WORKGROUP); };
  auto gfail = [&](int& n) -> bool { if ((++n & 63) == 0) { if (ctl[C_ABORT]) return true; if (n > GCAP) { ctl[C_ABORT] = 1; return true; } } return false; };

  if (wave == 1) { ctl[lane] = 0;
    wait_lgkm(); }
  __builtin_amdgcn_s_barrier();
  CBAR();

  if (wave == 0) {
    __builtin_amdgcn_s_setprio(3);
    const size_t cbase = ((size_t)(bat * H + head) * SK + (size_t)side * (SK / 2)) * DH * 2;
    const char* kb = (const char*)kc + cbase;
    const char* vb = (const char*)vc + cbase;
    const u32 voff = lane * 16;
    constexpr int PUB = (WIN - 1) % 8, LAG = (WIN - 1) / 8;
    int pub = 0, fr = 0;
    for (int u = 0; u < 2 * NU; ++u) {
      const int slot = u & (NS - 1);
      if (u >= NS) {
        const int need = u - NS + 1;
        if (fr < need) {
          if (ctl[C_FREE + slot] < need) {
            wait_vm<0>(); pub = u; ctl[C_LANDED] = u;
            lwait(C_FREE + slot, need, false);
          }
        }
        CBAR();
      }
      const char* src = (u < NU ? kb + (size_t)u * UNIT : vb + (size_t)(u - NU) * UNIT);
#define PIECE(i) do { if ((i) == 0) set_m0(lbase + slot * UNIT); if ((i) == 4) set_m0(lbase + slot * UNIT + 4096); \
        dma_nt<((i) & 3) * 1024>(voff, src + ((i) >> 2) * 4096); wait_vm<WIN>(); \
        if ((i) == PUB && u - LAG > pub) { pub = u - LAG; ctl[C_LANDED] = pub; } } while (0)
      PIECE(0); PIECE(1); PIECE(2); PIECE(3); PIECE(4); PIECE(5);
      fr = ctl[C_FREE + ((u + 1) & (NS - 1))];
      PIECE(6); PIECE(7);
#undef PIECE
    }
    wait_vm<16>(); if (pub < 2 * NU - 2) ctl[C_LANDED] = 2 * NU - 2;
    wait_vm<8>(); if (pub < 2 * NU - 1) ctl[C_LANDED] = 2 * NU - 1;
    wait_vm<0>(); ctl[C_LANDED] = 2 * NU;
    STAMP(3);
    wait_lgkm();
    return;
  }

  if (wave < 4) {
    const int ci = wave - 1, r = lane >> 4, c = lane & 15;
    int seen = 0, u = ci;
    float mx = -INFINITY;
#define FETCH(d) do { if (seen <= u) { lwait(C_LANDED, u + 1, true); seen = ctl[C_LANDED]; } CBAR(); \
      const int slot_ = u & (NS - 1); \
      _Pragma("unroll") for (int i_ = 0; i_ < 8; ++i_) d[i_] = *(LAS u32x4*)(L + slot_ * UNIT + i_ * 1024 + lane * 16); \
      CBAR(); ctl[C_FREE + slot_] = u + 1; CBAR(); } while (0)
    u32x4 qv;
    auto qk = [&](const u32x4 (&d)[8], int uu) {
      float s[8];
#pragma unroll
      for (int i = 0; i < 8; ++i) s[i] = dot2(d[i][3], qv[3], dot2(d[i][2], qv[2], dot2(d[i][1], qv[1], dot2(d[i][0], qv[0], 0.f))));
#define TR(HALF, CTRL, BIT) _Pragma("unroll") for (int h = 0; h < (HALF); ++h) { const bool up_ = (c & (BIT)) != 0; \
        const float keep_ = up_ ? s[h + (HALF)] : s[h], send_ = up_ ? s[h] : s[h + (HALF)]; s[h] = keep_ + dpp<CTRL>(send_); }
      TR(4, XOR8, 8) TR(2, XOR7, 4) TR(1, XOR2, 2)
#undef TR
      const float t = s[0] + dpp<XOR1>(s[0]);
      const u32 sb = bfbits(rb(t) * 0.08838834764831845f);
      mx = fmaxf(mx, lo2f(sb));
      *(LAS u16*)(L + L_SC + (uu * 32 + r * 8 + (c >> 1)) * 2) = (u16)sb;
    };
    {
      u32x4 pre[NPRE][8];
#pragma unroll
      for (int i = 0; i < NPRE; ++i) { FETCH(pre[i]); u += 3; }
      lwait(C_QRDY, 1, true);
      qv = *(LAS u32x4*)(L + L_Q + c * 16);
      if (ci == 0) STAMP(4);
#pragma unroll
      for (int i = 0; i < NPRE; ++i) qk(pre[i], ci + 3 * i);
    }
    u32x4 d[8];
    for (; u < NU; u += 3) { FETCH(d); qk(d, u); }
    mx = wave_max(mx);
    ctf[C_KMAX + ci] = mx;
    larrive(C_KCNT);
    lwait(C_PCNT, 4, true);
    float acc[8];
#pragma unroll
    for (int k = 0; k < 8; ++k) acc[k] = 0.f;
    for (; u < 2 * NU; u += 3) {
      const u32x4 pw = *(LAS u32x4*)(L + L_SC + ((u - NU) * 32 + r * 8) * 2);
      FETCH(d);
#pragma unroll
      for (int t = 0; t < 4; ++t)
#pragma unroll
        for (int k = 0; k < 4; ++k) {
          acc[2 * k] = dot2(__builtin_amdgcn_perm(d[2 * t + 1][k], d[2 * t][k], 0x05040100u), pw[t], acc[2 * k]);
          acc[2 * k + 1] = dot2(__builtin_amdgcn_perm(d[2 * t + 1][k], d[2 * t][k], 0x07060302u), pw[t], acc[2 * k + 1]);
        }
    }
#undef FETCH
    const f32x2 o2 = {rowsum4(acc[0], acc[2], acc[4], acc[6]), rowsum4(acc[1], acc[3], acc[5], acc[7])};
    *(LAS f32x2*)(L + L_OP + ci * 512 + (c * 8 + r * 2) * 4) = o2;
    wait_lgkm();
    __builtin_amdgcn_s_barrier();
    return;
  }

  const int w = wave - 4, kg = lane >> 4, c16 = lane & 15;
  const rsrc_t rs = __builtin_amdgcn_make_buffer_rsrc((void*)ws, 0, WS_END, 0x00020000);
  if (w == 0) STAMP(0);
  const bool mb = !side && g >= 4 && g < 12, f1 = !side && g >= 12 && g < 26, rr = (g & 7) == 1;
  const int jf = x * 7 + ((g - 12) >> 1), rbat = g >> 3;
  int ts = 0;
  auto tsync = [&](bool nap) { larrive(C_TS); ts += 4; lwait(C_TS, ts, nap); };
  auto tile_src = [&](const u16* pa, const u16* pb, int ld, int row0) -> const u16* {
    return ((lane & 4) ? pb : pa) + (lane & 3) * 8 + (size_t)(row0 + (lane >> 3)) * ld;
  };
  auto frag = [&](int buf, int t, int s) -> u32x4 {
    LAS unsigned char* p = L + buf + (s * 32 + kg * 8 + (c16 >> 2)) * 128 + (c16 & 2) * 32 + t * 16 + (c16 & 1) * 8;
    const u32x2 a = __builtin_bit_cast(u32x2, __builtin_amdgcn_ds_read_tr16_b64_v4i16((LAS s16x4*)p));
    const u32x2 b = __builtin_bit_cast(u32x2, __builtin_amdgcn_ds_read_tr16_b64_v4i16((LAS s16x4*)(p + 512)));
    return u32x4{a[0], a[1], b[0], b[1]};
  };
  auto pick = [&](const f32x4 (&a)[4]) -> float {
    const f32x4 v = kg == 0 ? a[0] : kg == 1 ? a[1] : kg == 2 ? a[2] : a[3];
    return rowsum4(v[0], v[1], v[2], v[3]);
  };
  auto lnorm = [&](u32x4 v, float (&n)[8]) {
#pragma unroll
    for (int k = 0; k < 4; ++k) { n[2 * k] = lo2f(v[k]); n[2 * k + 1] = hi2f(v[k]); }
    const float mean = wave_sum(((n[0] + n[1]) + (n[2] + n[3])) + ((n[4] + n[5]) + (n[6] + n[7]))) * (1.f / D);
    float q = 0.f;
#pragma unroll
    for (int k = 0; k < 8; ++k) { const float e = n[k] - mean; q = fmaf(e, e, q); }
    const float var = rb(wave_sum(q) * (1.f / D)), mr = rb(mean);
    const float inv = __builtin_amdgcn_rcpf(rb(__builtin_amdgcn_sqrtf(rb(var + 1e-5f))));
#pragma unroll
    for (int k = 0; k < 8; ++k) n[k] = rb(rb(n[k] - mr) * inv);
  };
  u32x4 W[16];
  auto modcols = [&](const u32x4 (&cv)[4]) -> float {
    f32x4 acc[4];
#pragma unroll
    for (int q = 0; q < 4; ++q) acc[q] = f32x4{0.f, 0.f, 0.f, 0.f};
#pragma unroll
    for (int t = 0; t < 4; ++t)
#pragma unroll
      for (int q = 0; q < 4; ++q) acc[q] = mfma(cv[t], W[4 * t + q], acc[q]);
    return rb(pick(acc));
  };
  auto load_c = [&](u32x4 (&cv)[4]) {
#pragma unroll
    for (int t = 0; t < 4; ++t) cv[t] = *(const GAS u32x4*)(cin + (lane & 3) * D + t * 128 + (c16 >> 2) * 32 + kg * 8);
  };
  auto silu_c = [&](u32x4 (&cv)[4]) {
#pragma unroll
    for (int t = 0; t < 4; ++t)
#pragma unroll
      for (int k = 0; k < 4; ++k) { const float p = lo2f(cv[t][k]), q = hi2f(cv[t][k]); cv[t][k] = pk2(p * sigmoidf_(p), q * sigmoidf_(q)); }
  };

  const int kq = g & 3, qb = (g >> 2) & 1;
  auto wq_tile = [&]() {
    const u16* p = tile_src(wq + head * DH + qb * 64, wq + head * DH + qb * 64 + 32, H * DH, kq * 128);
#pragma unroll
    for (int i = 0; i < 16; ++i) dma_at(p + (size_t)i * 8 * (H * DH), lbase + L_STG + i * 1024);
  };
  u32x2 ev = ld8(rs, WS_E);
#define TAGS() asm volatile("" : "+v"(ev)); const u32 tag0 = ev[0] + 1u, tag1 = ev[1] + 1u
  const u32 yoff = WS_Y + (bat * 64 + head * 2 + side) * 2048;
  if (w == 1) {
    const int band = x * 2 + (g & 1), sl = g >> 1;
    const u16* p = tile_src(wmod + band * 64, wmod + band * 64 + 32, 6 * D, sl * 32);
#pragma unroll
    for (int i = 0; i < 4; ++i) dma_at(p + (size_t)i * 8 * (6 * D), lbase + L_SC + i * 1024);
    u32x4 a = *(const GAS u32x4*)(cin + (c16 >> 2) * D + sl * 32 + kg * 8);
#pragma unroll
    for (int k = 0; k < 4; ++k) { const float p0 = lo2f(a[k]), p1 = hi2f(a[k]); a[k] = pk2(p0 * sigmoidf_(p0), p1 * sigmoidf_(p1)); }
    wait_vm<0>();
    TAGS();
    STAMP(6);
    const u32 po = WS_PG + (band * 16 + sl) * 2048 + (c16 >> 3) * 1024 + kg * 64 + (c16 & 7) * 8;
#pragma unroll
    for (int t = 0; t < 4; ++t) {
      const f32x4 acc = mfma(a, frag(L_SC, t, 0), f32x4{0.f, 0.f, 0.f, 0.f});
      st8(rs, po + t * 256, u32x2{__float_as_uint(acc[0]), (c16 & 1) ? tag1 : tag0});
    }
    STAMP(7);
  }
  if (w == 2 && (g & 3) == 3) {
    const int cg = x * 8 + (g >> 2);
    float sum = 0.f, sq = 0.f, xs[32];
#pragma unroll
    for (int i = 0; i < 4; ++i) {
      const u32x4 v = *(const GAS u32x4*)(xin + kg * D + i * 128 + c16 * 8);
#pragma unroll
      for (int k = 0; k < 4; ++k) { xs[8 * i + 2 * k] = lo2f(v[k]); xs[8 * i + 2 * k + 1] = hi2f(v[k]); }
    }
    const u32 xp = *(const GAS u32*)(xin + kg * D + cg * 8 + (c16 & 3) * 2);
#pragma unroll
    for (int i = 0; i < 32; ++i) sum += xs[i];
    const float mean = row_sum(sum) * (1.f / D);
#pragma unroll
    for (int i = 0; i < 32; ++i) { const float e = xs[i] - mean; sq = fmaf(e, e, sq); }
    const float var = rb(row_sum(sq) * (1.f / D)), mr = rb(mean);
    const float inv = __builtin_amdgcn_rcpf(rb(__builtin_amdgcn_sqrtf(rb(var + 1e-5f))));
    float n0 = rb(rb(lo2f(xp) - mr) * inv), n1 = rb(rb(hi2f(xp) - mr) * inv);
    asm volatile("" : "+v"(n0), "+v"(n1));
    TAGS();
    __builtin_amdgcn_s_sleep(ADLY);
    const u32 pi = WS_PG + ((cg >> 3) * 16 + (c16 >> 2)) * 2048 + (cg & 7) * 256 + kg * 64 + (c16 & 3) * 16;
    u32x4 v[8]; bool done[8]; int n = 0;
#pragma unroll
    for (int i = 0; i < 8; ++i) done[i] = false;
    for (;;) {
      CBAR();
      bool all = true;
#pragma unroll
      for (int i = 0; i < 8; ++i) if (!done[i]) v[i] = ld16(rs, pi + (i & 3) * 8192 + (i >> 2) * 262144);
#pragma unroll
      for (int i = 0; i < 8; ++i) { done[i] = v[i][1] == tag0 && v[i][3] == tag1; all &= done[i]; }
      if (__all(all) || gfail(n)) break;
      if (PSLP) __builtin_amdgcn_s_sleep(PSLP);
    }
    STAMP(5);
    float mv[4];
#pragma unroll
    for (int k = 0; k < 4; ++k) {
      const int i0 = (k >> 1) * 4, e = (k & 1) * 2;
      float t = (__uint_as_float(v[i0][e]) + __uint_as_float(v[i0 + 1][e])) + (__uint_as_float(v[i0 + 2][e]) + __uint_as_float(v[i0 + 3][e]));
      t += dpp<XOR8>(t); t += dpp<ROR4>(t);
      mv[k] = rb(t);
    }
    const u32 pr = pk2(rb(n0 * rb(1.f + mv[2])) + mv[0], rb(n1 * rb(1.f + mv[3])) + mv[1]);
    if (c16 < 4) st8(rs, WS_HG + cg * 128 + kg * 32 + c16 * 8, u32x2{pr, (c16 & 1) ? tag1 : tag0});
    STAMP(29);
  }
  if (w == 0) {
    wq_tile();
    wait_vm<0>();
    TAGS();
#pragma unroll
    for (int i = 0; i < 16; ++i) W[i] = frag(L_STG, i >> 2, i & 3);
#pragma unroll
    for (int i = 0; i < 16; ++i) asm volatile("" : "+v"(W[i]));
    STAMP(8);
    __builtin_amdgcn_s_sleep(HDLY);
    u32x4 v0, v1; int n = 0;
    const u32 ho = WS_HG + (kq * 16 + (c16 >> 2) * 4 + kg) * 128 + (lane & 3) * 32;
    bool hd = false;
    for (;;) {
      CBAR();
      if (!hd) { v0 = ld16(rs, ho); v1 = ld16(rs, ho + 16); }
      hd = v0[1] == tag0 && v0[3] == tag1 && v1[1] == tag0 && v1[3] == tag1;
      if (__all(hd) || gfail(n)) break;
      if (PSLP) __builtin_amdgcn_s_sleep(PSLP);
    }
    STAMP(9);
    const u32x4 a = {v0[0], v0[2], v1[0], v1[2]};
    float qp[4];
#pragma unroll
    for (int t = 0; t < 4; ++t) {
      f32x4 acc[4];
#pragma unroll
      for (int q = 0; q < 4; ++q) acc[q] = mfma(a, W[4 * t + q], f32x4{0.f, 0.f, 0.f, 0.f});
      qp[t] = pick(acc);
    }
    const u32 qo = WS_QG + (head * 4 + kg) * 4096 + (g & 7) * 512 + c16 * 16;
    st16(rs, qo, u32x4{__float_as_uint(qp[0]), tag0, __float_as_uint(qp[1]), tag1});
    st16(rs, qo + 256, u32x4{__float_as_uint(qp[2]), tag0, __float_as_uint(qp[3]), tag1});
    STAMP(28);
    if (QDLY) __builtin_amdgcn_s_sleep(QDLY);
    const u32 qi = WS_QG + (head * 4 + bat) * 4096 + (lane >> 5) * 2048 + (lane & 31) * 16;
    u32x4 v[4]; bool done[4] = {false, false, false, false};
    for (;;) {
      CBAR();
#pragma unroll
      for (int i = 0; i < 4; ++i) if (!done[i]) v[i] = ld16(rs, qi + i * 512);
#pragma unroll
      for (int i = 0; i < 4; ++i) done[i] = v[i][1] == tag0 && v[i][3] == tag1;
      if (__all(done[0] && done[1] && done[2] && done[3]) || gfail(n)) break;
      if (PSLP) __builtin_amdgcn_s_sleep(PSLP);
    }
    const int col = (lane >> 5) * 64 + (c16 >> 3) * 32 + (kg & 1) * 16 + (c16 & 7);
    *(LAS u16*)(L + L_Q + col * 2) = (u16)bfbits((__uint_as_float(v[0][0]) + __uint_as_float(v[1][0])) + (__uint_as_float(v[2][0]) + __uint_as_float(v[3][0])));
    *(LAS u16*)(L + L_Q + col * 2 + 16) = (u16)bfbits((__uint_as_float(v[0][2]) + __uint_as_float(v[1][2])) + (__uint_as_float(v[2][2]) + __uint_as_float(v[3][2])));
    wait_lgkm();
    ctl[C_QRDY] = 1;
  }
  TAGS();
  tsync(true);
  {
    const u32x4 sv = {SENT, SENT, SENT, SENT};
    if (w == 1) { st16(rs, yoff + lane * 16, sv); st16(rs, yoff + 1024 + lane * 16, sv); }
    if (w == 2) { if (lane == 0) st16(rs, WS_ML + blk * 128, sv);
      if (rr) st16(rs, WS_X1 + (lane >> 3) * 4096 + rbat * 1024 + x * 128 + (lane & 7) * 16, sv); }
    if (f1) { st16(rs, WS_Z + (w * 64 + jf) * 2048 + lane * 16, sv); st16(rs, WS_Z + (w * 64 + jf) * 2048 + 1024 + lane * 16, sv); }
  }
  const int mcol = 2 * D + (x * 4 + ((g - 4) >> 1)) * 64;
  if (mb || f1) {
    const int ld = f1 ? F : 6 * D;
    const u16* p = tile_src(f1 ? wg + jf * 32 : wmod + mcol, f1 ? wu + jf * 32 : wmod + mcol + 32, ld, w * 32);
#pragma unroll
    for (int t = 0; t < 4; ++t) {
      for (int i = 0; i < w; ++i) __builtin_amdgcn_s_sleep(SLP_BAND);
#pragma unroll
      for (int i = 0; i < 4; ++i) { dma_at(p + (size_t)(t * 128 + i * 8) * ld, lbase + L_STG + w * 4096 + i * 1024); __builtin_amdgcn_s_sleep(4 * SLP_BAND); }
      wait_vm<0>();
      tsync(true);
#pragma unroll
      for (int s = 0; s < 4; ++s) W[4 * t + s] = frag(L_STG, w, s);
      tsync(true);
#pragma unroll
      for (int s = 0; s < 4; ++s) asm volatile("" : "+v"(W[4 * t + s]));
    }
    if (mb) {
      u32x4 cv[4]; load_c(cv); silu_c(cv);
      const u32 mine = bfbits(modcols(cv)), pr = mine | (dppu<XOR1>(mine) << 16);
      if ((c16 & 1) == 0) st4(rs, WS_MOD + (kg * 6 * D + mcol + (c16 >> 3) * 32 + w * 8 + (c16 & 7)) * 2, pr);
    }
  }
  wait_vm<0>();
  tsync(true);
  if (w == 0) { if (lane == 0) st8(rs, WS_FLAG + blk * 8, u32x2{tag0, tag1}); STAMP(10); }
  lwait(C_KCNT, 3, true);
  const float m = fmaxf(fmaxf(ctf[C_KMAX], ctf[C_KMAX + 1]), ctf[C_KMAX + 2]);
  {
    float l = 0.f;
#pragma unroll
    for (int i = 0; i < 2; ++i) {
      LAS u32x4* p = (LAS u32x4*)(L + L_SC + (i * 256 + w * 64 + lane) * 16);
      u32x4 v = *p;
#pragma unroll
      for (int k = 0; k < 4; ++k) {
        const float a = __builtin_amdgcn_exp2f((lo2f(v[k]) - m) * 1.4426950408889634f), b = __builtin_amdgcn_exp2f((hi2f(v[k]) - m) * 1.4426950408889634f);
        l += a + b; v[k] = pk2(a, b);
      }
      *p = v;
    }
    ctf[C_L + w] = wave_sum(l);
    larrive(C_PCNT);
  }
  float lsum = 0.f;
  if (w == 0) {
    lwait(C_PCNT, 4, false);
    lsum = (ctf[C_L] + ctf[C_L + 1]) + (ctf[C_L + 2] + ctf[C_L + 3]);
    if (lane == 0) st8(rs, WS_ML + blk * 128, u32x2{clean(__float_as_uint(m)), clean(__float_as_uint(lsum))});
  }
  u32x4 B[4][8], t0[8];
  auto wperm = [&](int s) {
#pragma unroll
    for (int j = 0; j < 8; ++j)
#pragma unroll
      for (int k = 0; k < 4; ++k) B[s][j][k] = __builtin_amdgcn_perm(t0[2 * k + 1][j >> 1], t0[2 * k][j >> 1], (j & 1) ? 0x07060302u : 0x05040100u);
#pragma unroll
    for (int j = 0; j < 8; ++j) asm volatile("" : "+v"(B[s][j]));
  };
  {
    const u16* p = wo + (size_t)(head * DH + kg * 8) * D + w * 128 + c16 * 8;
    for (int i = 0; i < w; ++i) __builtin_amdgcn_s_sleep(SLP_WO);
#pragma unroll
    for (int s = 0; s < 4; ++s) {
#pragma unroll
      for (int j = 0; j < 8; ++j) { CBAR(); t0[j] = *(const GAS u32x4*)(p + (size_t)(s * 32 + j) * D); CBAR(); __builtin_amdgcn_s_sleep(4 * SLP_WO); }
      wperm(s);
    }
  }
  if (w == 0) {
    int n = 0;
    for (;;) {
      CBAR();
      const u32x4 a = ld16(rs, WS_FLAG + lane * 32), b = ld16(rs, WS_FLAG + lane * 32 + 16);
      const bool ok = a[0] == tag0 && a[1] == tag1 && a[2] == tag0 && a[3] == tag1 && b[0] == tag0 && b[1] == tag1 && b[2] == tag0 && b[3] == tag1;
      if (__all(ok) || gfail(n)) break;
      __builtin_amdgcn_s_sleep(32);
    }
    ctl[C_INIT] = 1;
    if (blk == 0 && lane == 0) st8(rs, WS_E, u32x2{tag0, tag1});
    u32x2 o;
    for (;;) {
      CBAR();
      o = ld8(rs, WS_ML + (blk ^ 8) * 128);
      if (__all(o[0] != SENT && o[1] != SENT) || gfail(n)) break;
      __builtin_amdgcn_s_sleep(32);
    }
    const float m2 = __uint_as_float(o[0]), l2 = __uint_as_float(o[1]), M = fmaxf(m, m2);
    const float e1 = __builtin_amdgcn_exp2f((m - M) * 1.4426950408889634f), e2 = __builtin_amdgcn_exp2f((m2 - M) * 1.4426950408889634f);
    ctf[C_SCALE] = e1 / (lsum * e1 + l2 * e2);
    STAMP(11);
  }
  lwait(C_INIT, 1, true);
  u32x4 ma = {0u, 0u, 0u, 0u}, mc = ma;
  if (f1) { ma = ld16(rs, WS_MOD + (w * 6 * D + 3 * D + lane * 8) * 2); mc = ld16(rs, WS_MOD + (w * 6 * D + 4 * D + lane * 8) * 2); }
  if (rr) { ma[0] = ld4(rs, WS_MOD + (rbat * 6 * D + 2 * D + x * 64 + (lane & ~1)) * 2); ma[1] = ld4(rs, WS_MOD + (rbat * 6 * D + 5 * D + x * 64 + (lane & ~1)) * 2);
            mc[0] = *(const GAS u16*)(xin + rbat * D + x * 64 + lane); }
  asm volatile("s_waitcnt vmcnt(0) lgkmcnt(0)" : "+v"(ma), "+v"(mc) :: "memory");
  __builtin_amdgcn_s_barrier();
  CBAR();
  if (w == 0) STAMP(12);
  {
    const float sc = ctf[C_SCALE];
    const LAS f32x2* op = (const LAS f32x2*)(L + L_OP) + lane;
    const f32x2 o = ((op[0] + op[64]) + op[128]) * sc;
    const u32 hi = pk2(o[0], o[1]), lo = pk2(o[0] - lo2f(hi), o[1] - hi2f(hi));
    *(LAS u32*)(L + L_AROW + w * 512 + lane * 4) = hi;
    *(LAS u32*)(L + L_AROW + w * 512 + 256 + lane * 4) = lo;
    f32x4 acc[8];
#pragma unroll
    for (int j = 0; j < 8; ++j) acc[j] = f32x4{0.f, 0.f, 0.f, 0.f};
#pragma unroll
    for (int s = 0; s < 4; ++s) {
      const u32x4 a = *(LAS u32x4*)(L + L_AROW + w * 512 + (lane & 1) * 256 + s * 64 + kg * 16);
#pragma unroll
      for (int j = 0; j < 8; ++j) acc[j] = mfma(a, B[s][j], acc[j]);
    }
    u32x4 y;
#pragma unroll
    for (int k = 0; k < 4; ++k) y[k] = clean(__float_as_uint(kg == 0 ? acc[k][0] + acc[k][1] : acc[k + 4][0] + acc[k + 4][1]));
    if (kg < 2) st16(rs, yoff + (w * 128 + c16 * 8 + kg * 4) * 4, y);
  }
  if (w == 0) STAMP(13);
  if (rr) {
    float base = lo2f(mc[0]);
    int n = 0;
#pragma nounroll
    for (int pass = 0; pass < 2; ++pass) {
      const u32 off = (pass ? WS_Z : WS_Y) + (rbat * 64 + w * 16 + kg) * 2048 + (x * 64 + c16 * 4) * 4;
      const bool cut = pass && w == 3;
      u32x4 v[4]; bool done[4] = {false, false, cut, cut};
      v[2] = v[3] = u32x4{0u, 0u, 0u, 0u};
      for (;;) {
        CBAR();
#pragma unroll
        for (int i = 0; i < 4; ++i) if (!done[i]) v[i] = ld16(rs, off + i * 8192);
#pragma unroll
        for (int i = 0; i < 4; ++i) done[i] = ok4(v[i]);
        if (__all(done[0] && done[1] && done[2] && done[3]) || gfail(n)) break;
      }
      if (w == 0 && pass) STAMP(18);
      const f32x4 s = (__builtin_bit_cast(f32x4, v[0]) + __builtin_bit_cast(f32x4, v[1])) + (__builtin_bit_cast(f32x4, v[2]) + __builtin_bit_cast(f32x4, v[3]));
      *(LAS float*)(L + L_PART + (w * 64 + c16 * 4 + kg) * 4) = rowsum4(s[0], s[1], s[2], s[3]);
      wait_lgkm();
      __builtin_amdgcn_s_barrier();
      CBAR();
      if (w == 0) {
        const LAS float* pp = (const LAS float*)(L + L_PART) + lane;
        const float a = rb((pp[0] + pp[64]) + (pp[128] + pp[192]));
        const u32 gw = pass ? ma[1] : ma[0];
        const u32 mine = bfbits(base + rb(((lane & 1) ? hi2f(gw) : lo2f(gw)) * a));
        base = lo2f(mine);
        const u32 pr0 = mine | (dppu<XOR1>(mine) << 16);
        if (pass) { if ((lane & 1) == 0) *(GAS u32*)(out + rbat * D + x * 64 + lane) = pr0; STAMP(19); }
        else {
          const u32 pr = clean(pr0);
          const u32x4 oa = {dppu<BCAST + 0>(pr), dppu<BCAST + 2>(pr), dppu<BCAST + 4>(pr), dppu<BCAST + 6>(pr)};
          const u32x4 ob = {dppu<BCAST + 8>(pr), dppu<BCAST + 10>(pr), dppu<BCAST + 12>(pr), dppu<BCAST + 14>(pr)};
          st16(rs, WS_X1 + (lane & 7) * 4096 + rbat * 1024 + x * 128 + (lane >> 3) * 16, (lane & 8) ? ob : oa);
          STAMP(14);
        }
      }
      if (pass == 0) {
        for (;;) {
          CBAR();
          const u32 t = ld4(rs, WS_X1 + x * 4096 + kg * 1024 + x * 128);
          if (__all(t != SENT) || gfail(n)) break;
        }
        if (w == 0) STAMP(17);
        __builtin_amdgcn_s_sleep(ZDLY);
      }
    }
  } else if (f1) {
    {
      const u16* p = wd + (size_t)(jf * 32 + kg * 8) * D + w * 128 + c16 * 8;
#pragma unroll
      for (int j = 0; j < 8; ++j) t0[j] = *(const GAS u32x4*)(p + (size_t)j * D);
      wperm(0);
    }
    u32x4 v; bool done = false; int n = 0;
    for (;;) {
      CBAR();
      if (!done) v = ld16(rs, WS_X1 + x * 4096 + w * 1024 + lane * 16);
      done = ok4(v);
      if (__all(done) || gfail(n)) break;
    }
    float nn[8]; lnorm(v, nn);
    u32x4 h2;
#pragma unroll
    for (int k = 0; k < 4; ++k) h2[k] = pk2(rb(nn[2 * k] * rb(1.f + lo2f(mc[k]))) + lo2f(ma[k]), rb(nn[2 * k + 1] * rb(1.f + hi2f(mc[k]))) + hi2f(ma[k]));
    *(LAS u32x4*)(L + L_STG + w * 1040 + lane * 16) = h2;
    wait_lgkm();
    __builtin_amdgcn_s_barrier();
    CBAR();
    if (w == 0) STAMP(15);
    u32x4 a4[4];
#pragma unroll
    for (int t = 0; t < 4; ++t) a4[t] = *(LAS u32x4*)(L + L_STG + (lane & 3) * 1040 + (t * 128 + (c16 >> 2) * 32 + kg * 8) * 2);
    const float gv = modcols(a4), uv = dpp<XOR8>(gv);
    const float ge = rb(gv * sigmoidf_(1.5957691216057308f * (gv + 0.044715f * gv * gv * gv)));
    if (c16 < 8) *(LAS u16*)(L + L_PART + kg * 64 + (w * 8 + c16) * 2) = (u16)bfbits(ge * uv);
    wait_lgkm();
    __builtin_amdgcn_s_barrier();
    CBAR();
    const u32x4 a = *(LAS u32x4*)(L + L_PART + ((c16 & 1) * 2 + (c16 >> 3)) * 64 + kg * 16);
    f32x4 acc[8];
#pragma unroll
    for (int j = 0; j < 8; ++j) acc[j] = mfma(a, B[0][j], f32x4{0.f, 0.f, 0.f, 0.f});
#pragma unroll
    for (int i = 0; i < 2; ++i) {
      u32x4 z;
#pragma unroll
      for (int k = 0; k < 4; ++k) z[k] = clean(__float_as_uint((kg & 1) ? acc[k + 4][i] : acc[k][i]));
      st16(rs, WS_Z + ((2 * i + (kg >> 1)) * 64 + jf) * 2048 + (w * 128 + c16 * 8 + (kg & 1) * 4) * 4, z);
    }
    if (w == 0) STAMP(16);
  }
  if (w == 0) STAMP_OUT();
}

extern "C" void kernel_launch(void* const* d_in, const int* in_sizes, int n_in, void* d_out, int out_size, void* d_ws, size_t ws_size,
                              hipStream_t stream) {
  adaln_block_kernel<<<256, 512, 0, stream>>>((const u16*)d_in[0], (const u16*)d_in[1], (const u16*)d_in[2], (const u16*)d_in[3], (const u16*)d_in[4],
      (const u16*)d_in[6], (const u16*)d_in[7], (const u16*)d_in[8], (const u16*)d_in[9], (const u16*)d_in[10], (u16*)d_out, (unsigned char*)d_ws);
}
```

```cpp
#define STAMPS 1
#define DIAG 0
#define DBLK 8
#define DSCALE 1
#include <hip/hip_runtime.h>
#include <hip/hip_bf16.h>
#include <cstdint>

typedef unsigned short u16;
typedef unsigned u32;
typedef unsigned long long u64;
using u32x2 = __attribute__((ext_vector_type(2))) unsigned;
using u32x4 = __attribute__((ext_vector_type(4))) unsigned;
using f32x2 = __attribute__((ext_vector_type(2))) float;
using f32x4 = __attribute__((ext_vector_type(4))) float;
using bf16x2 = __attribute__((ext_vector_type(2))) __bf16;
using bf16x8 = __attribute__((ext_vector_type(8))) __bf16;
using s16x4 = __attribute__((ext_vector_type(4))) short;
typedef __amdgpu_buffer_rsrc_t rsrc_t;
#define LAS __attribute__((address_space(3)))
#define GAS __attribute__((address_space(1)))
#define DEV __device__ __forceinline__

#define WIN 19
#define ZDLY 30
#define ADLY 60
#define HDLY 110
#define PSLP 20
#define QDLY 45
#define NPRE 6
#define SLP_BAND 11
#define SLP_WO 7

constexpr int D = 512, H = 32, SK = 8192, DH = 128, F = 1792;
constexpr int NS = 16, UNIT = 8192, NU = 128;
constexpr u32 SENT = 0xFFFFFFFFu;
constexpr int GCAP = 1 << 21, LCAP = 1 << 23;

constexpr u32 WS_E = 0, WS_FLAG = 4096, WS_HG = 8192, WS_MOD = 32768, WS_ML = 65536, WS_X1 = 131072, WS_QG = 524288,
              WS_Y = 1048576, WS_Z = 1572864, WS_PG = 2097152, WS_END = 2621440;
constexpr int L_SC = NS * UNIT, L_STG = L_SC + 8192, L_Q = L_STG + 16384, L_OP = L_Q + 256, L_AROW = L_OP + 1536,
              L_PART = L_AROW + 2048, L_NV = L_PART + 1024, L_CTL = L_NV + 512, L_ST = L_CTL + 256, L_END = L_ST + 256;
static_assert(L_END <= 163840, "LDS");
enum { C_LANDED = 0, C_ABORT, C_QRDY, C_KCNT, C_PCNT, C_TS, C_INIT, C_SCALE, C_KMAX = 8, C_AFREE = 11, C_L = 12, C_FREE = 32 };

DEV float lo2f(u32 v) { return __builtin_bit_cast(float, v << 16); }
DEV float hi2f(u32 v) { return __builtin_bit_cast(float, v & 0xffff0000u); }
DEV u32 pk2(float a, float b) { bf16x2 r = {(__bf16)a, (__bf16)b}; return __builtin_bit_cast(u32, r); }
DEV u32 bfbits(float a) { return (u32)__builtin_bit_cast(u16, (__bf16)a); }
DEV float rb(float a) { return lo2f(bfbits(a)); }
DEV float dot2(u32 a, u32 b, float c) { return __builtin_amdgcn_fdot2_f32_bf16(__builtin_bit_cast(bf16x2, a), __builtin_bit_cast(bf16x2, b), c, false); }
template <int CTRL> DEV u32 dppu(u32 x) { return (u32)__builtin_amdgcn_mov_dpp((int)x, CTRL, 0xf, 0xf, true); }
template <int CTRL> DEV float dpp(float x) { return __builtin_bit_cast(float, dppu<CTRL>(__builtin_bit_cast(u32, x))); }
constexpr int XOR1 = 0xB1, XOR2 = 0x4E, XOR7 = 0x141, XOR8 = 0x128, ROR4 = 0x124, BCAST = 0x150;
DEV float tsum32(float a, float b) { auto s = __builtin_amdgcn_permlane32_swap(__float_as_uint(a), __float_as_uint(b), false, false); return __uint_as_float(s[0]) + __uint_as_float(s[1]); }
DEV float tsum16(float a, float b) { auto s = __builtin_amdgcn_permlane16_swap(__float_as_uint(a), __float_as_uint(b), false, false); return __uint_as_float(s[0]) + __uint_as_float(s[1]); }
DEV float row_sum(float x) { x += dpp<XOR1>(x); x += dpp<XOR2>(x); x += dpp<XOR7>(x); return x + dpp<XOR8>(x); }
DEV float wave_sum(float x) { x = row_sum(x); x = tsum16(x, x); return tsum32(x, x); }
DEV float wave_max(float x) {
  x = fmaxf(x, dpp<XOR1>(x)); x = fmaxf(x, dpp<XOR2>(x)); x = fmaxf(x, dpp<XOR7>(x)); x = fmaxf(x, dpp<XOR8>(x));
  auto s = __builtin_amdgcn_permlane16_swap(__float_as_uint(x), __float_as_uint(x), false, false); x = fmaxf(__uint_as_float(s[0]), __uint_as_float(s[1]));
  auto t = __builtin_amdgcn_permlane32_swap(__float_as_uint(x), __float_as_uint(x), false, false); return fmaxf(__uint_as_float(t[0]), __uint_as_float(t[1]));
}
DEV float rowsum4(float v0, float v1, float v2, float v3) { return tsum16(tsum32(v0, v2), tsum32(v1, v3)); }
DEV f32x4 mfma(u32x4 a, u32x4 b, f32x4 c) { return __builtin_amdgcn_mfma_f32_16x16x32_bf16(__builtin_bit_cast(bf16x8, a), __builtin_bit_cast(bf16x8, b), c, 0, 0, 0); }
DEV float sigmoidf_(float v) { return __builtin_amdgcn_rcpf(1.f + __builtin_amdgcn_exp2f(-1.4426950408889634f * v)); }
DEV u32 clean(u32 v) { return v < SENT ? v : SENT - 1u; }

#define CBAR() asm volatile("" ::: "memory")
template <int N> DEV void wait_vm() { asm volatile("s_waitcnt vmcnt(%0)" :: "i"(N) : "memory"); }
DEV void wait_lgkm() { asm volatile("s_waitcnt lgkmcnt(0)" ::: "memory"); }
DEV u32x4 ld16(rsrc_t r, u32 off) { return __builtin_bit_cast(u32x4, __builtin_amdgcn_raw_buffer_load_b128(r, off, 0, 16)); }
DEV u32x2 ld8(rsrc_t r, u32 off) { return __builtin_bit_cast(u32x2, __builtin_amdgcn_raw_buffer_load_b64(r, off, 0, 16)); }
DEV u32 ld4(rsrc_t r, u32 off) { return __builtin_amdgcn_raw_buffer_load_b32(r, off, 0, 16); }
DEV void st16(rsrc_t r, u32 off, u32x4 v) { __builtin_amdgcn_raw_buffer_store_b128(v, r, off, 0, 16); }
DEV void st8(rsrc_t r, u32 off, u32x2 v) { __builtin_amdgcn_raw_buffer_store_b64(v, r, off, 0, 16); }
DEV void st4(rsrc_t r, u32 off, u32 v) { __builtin_amdgcn_raw_buffer_store_b32(v, r, off, 0, 16); }
DEV bool ok4(u32x4 v) { return v[0] != SENT && v[1] != SENT && v[2] != SENT && v[3] != SENT; }

template <int OFF> DEV void dma_nt(u32 voff, const void* base) { asm volatile("global_load_lds_dwordx4 %0, %1 offset:%2 nt" :: "v"(voff), "s"(base), "i"(OFF) : "memory"); }
DEV void set_m0(u32 v) { asm volatile("s_mov_b32 m0, %0\n\ts_nop 0" :: "s"(v) : "memory"); }
DEV void dma_at(const void* p, u32 m0v) { asm volatile("s_mov_b32 m0, %1\n\ts_nop 0\n\tglobal_load_lds_dwordx4 %0, off" :: "v"(p), "s"(m0v) : "memory"); }

__global__ void __launch_bounds__(512, 2) adaln_block_kernel(const u16* __restrict__ xin, const u16* __restrict__ kc, const u16* __restrict__ vc,
    const u16* __restrict__ wq, const u16* __restrict__ wo, const u16* __restrict__ wg, const u16* __restrict__ wu, const u16* __restrict__ wd,
    const u16* __restrict__ cin, const u16* __restrict__ wmod, u16* __restrict__ out, unsigned char* ws) {
  __shared__ __attribute__((aligned(16))) unsigned char lds_raw[L_END];
  LAS unsigned char* L = (LAS unsigned char*)lds_raw;
  volatile LAS int* ctl = (volatile LAS int*)(L + L_CTL);
  volatile LAS float* ctf = (volatile LAS float*)(L + L_CTL);
  const u32 lbase = (u32)(size_t)L;
  const int tid = threadIdx.x, lane = tid & 63, wave = __builtin_amdgcn_readfirstlane(tid >> 6);
  const int blk = blockIdx.x, x = blk & 7, g = blk >> 3;
  const int head = (g >> 3) * 8 + x, bat = (g >> 1) & 3, side = g & 1;
  volatile LAS u64* stp = (volatile LAS u64*)(L + L_ST);
#define STAMP(i) do { const u64 t_ = __builtin_amdgcn_s_memrealtime(); if (lane == 0) stp[i] = t_; } while (0)
#define STAMP_OUT() do { wait_lgkm(); if (blk == DBLK) { const u64 t0_ = stp[0], tx_ = stp[DIAG]; while (__builtin_amdgcn_s_memrealtime() < tx_ + 15000 + (tx_ - t0_) * (DSCALE - 1)) __builtin_amdgcn_s_sleep(8); } } while (0)
  auto lwait = [&](int idx, int want, bool nap) {
    int n = 0;
    while (ctl[idx] < want) {
      if (nap) __builtin_amdgcn_s_sleep(1);
      if ((++n & 255) == 0) { if (ctl[C_ABORT]) break; if (n > LCAP) { ctl[C_ABORT] = 1; break; } }
    }
    CBAR();
  };
  auto larrive = [&](int idx) { wait_lgkm(); if (lane == 0) __hip_atomic_fetch_add((LAS int*)(L + L_CTL) + idx, 1, __ATOMIC_RELAXED, __HIP_MEMORY_SCOPE_WORKGROUP); };
  auto gfail = [&](int& n) -> bool { if ((++n & 63) == 0) { if (ctl[C_ABORT]) return true; if (n > GCAP) { ctl[C_ABORT] = 1; return true; } } return false; };

  if (wave == 1) { ctl[lane] = 0;
    ((volatile LAS int*)(L + L_ST))[lane] = 0;
    wait_lgkm(); }
  __builtin_amdgcn_s_barrier();
  CBAR();

  if (wave == 0) {
    __builtin_amdgcn_s_setprio(3);
    const size_t cbase = ((size_t)(bat * H + head) * SK + (size_t)side * (SK / 2)) * DH * 2;
    const char* kb = (const char*)kc + cbase;
    const char* vb = (const char*)vc + cbase;
    const u32 voff = lane * 16;
    constexpr int PUB = (WIN - 1) % 8, LAG = (WIN - 1) / 8;
    int pub = 0, fr = 0;
    for (int u = 0; u < 2 * NU; ++u) {
      const int slot = u & (NS - 1);
      if (u >= NS) {
        const int need = u - NS + 1;
        if (fr < need) {
          if (ctl[C_FREE + slot] < need) {
            wait_vm<0>(); pub = u; ctl[C_LANDED] = u;
            lwait(C_FREE + slot, need, false);
            if (lane == 0) stp[u < NU ? 30 : 31] = stp[u < NU ? 30 : 31] + 1;
          }
        }
        CBAR();
      }
      const char* src = (u < NU ? kb + (size_t)u * UNIT : vb + (size_t)(u - NU) * UNIT);
#define PIECE(i) do { if ((i) == 0) set_m0(lbase + slot * UNIT); if ((i) == 4) set_m0(lbase + slot * UNIT + 4096); \
        dma_nt<((i) & 3) * 1024>(voff, src + ((i) >> 2) * 4096); wait_vm<WIN>(); \
        if ((i) == PUB && u - LAG > pub) { pub = u - LAG; ctl[C_LANDED] = pub; } } while (0)
      PIECE(0); PIECE(1); PIECE(2); PIECE(3); PIECE(4); PIECE(5);
      fr = ctl[C_FREE + ((u + 1) & (NS - 1))];
      PIECE(6); PIECE(7);
#undef PIECE
      if (u == 3) STAMP(1);
      if (u == NU) STAMP(2);
      if ((u & 31) == 31) STAMP(20 + (u >> 5));
    }
    wait_vm<16>(); if (pub < 2 * NU - 2) ctl[C_LANDED] = 2 * NU - 2;
    wait_vm<8>(); if (pub < 2 * NU - 1) ctl[C_LANDED] = 2 * NU - 1;
    wait_vm<0>(); ctl[C_LANDED] = 2 * NU;
    STAMP(3);
    wait_lgkm();
    return;
  }

  if (wave < 4) {
    const int ci = wave - 1, r = lane >> 4, c = lane & 15;
    int seen = 0, u = ci;
    float mx = -INFINITY;
#define FETCH(d) do { if (seen <= u) { lwait(C_LANDED, u + 1, true); seen = ctl[C_LANDED]; } CBAR(); \
      const int slot_ = u & (NS - 1); \
      _Pragma("unroll") for (int i_ = 0; i_ < 8; ++i_) d[i_] = *(LAS u32x4*)(L + slot_ * UNIT + i_ * 1024 + lane * 16); \
      CBAR(); ctl[C_FREE + slot_] = u + 1; CBAR(); } while (0)
    u32x4 qv;
    auto qk = [&](const u32x4 (&d)[8], int uu) {
      float s[8];
#pragma unroll
      for (int i = 0; i < 8; ++i) s[i] = dot2(d[i][3], qv[3], dot2(d[i][2], qv[2], dot2(d[i][1], qv[1], dot2(d[i][0], qv[0], 0.f))));
#define TR(HALF, CTRL, BIT) _Pragma("unroll") for (int h = 0; h < (HALF); ++h) { const bool up_ = (c & (BIT)) != 0; \
        const float keep_ = up_ ? s[h + (HALF)] : s[h], send_ = up_ ? s[h] : s[h + (HALF)]; s[h] = keep_ + dpp<CTRL>(send_); }
      TR(4, XOR8, 8) TR(2, XOR7, 4) TR(1, XOR2, 2)
#undef TR
      const float t = s[0] + dpp<XOR1>(s[0]);
      const u32 sb = bfbits(rb(t) * 0.08838834764831845f);
      mx = fmaxf(mx, lo2f(sb));
      *(LAS u16*)(L + L_SC + (uu * 32 + r * 8 + (c >> 1)) * 2) = (u16)sb;
    };
    {
      u32x4 pre[NPRE][8];
#pragma unroll
      for (int i = 0; i < NPRE; ++i) { FETCH(pre[i]); u += 3; }
      lwait(C_QRDY, 1, true);
      qv = *(LAS u32x4*)(L + L_Q + c * 16);
      if (ci == 0) STAMP(4);
#pragma unroll
      for (int i = 0; i < NPRE; ++i) qk(pre[i], ci + 3 * i);
    }
    u32x4 d[8];
    for (; u < NU; u += 3) { FETCH(d); qk(d, u); }
    mx = wave_max(mx);
    ctf[C_KMAX + ci] = mx;
    larrive(C_KCNT);
    lwait(C_PCNT, 4, true);
    float acc[8];
#pragma unroll
    for (int k = 0; k < 8; ++k) acc[k] = 0.f;
    for (; u < 2 * NU; u += 3) {
      const u32x4 pw = *(LAS u32x4*)(L + L_SC + ((u - NU) * 32 + r * 8) * 2);
      FETCH(d);
#pragma unroll
      for (int t = 0; t < 4; ++t)
#pragma unroll
        for (int k = 0; k < 4; ++k) {
          acc[2 * k] = dot2(__builtin_amdgcn_perm(d[2 * t + 1][k], d[2 * t][k], 0x05040100u), pw[t], acc[2 * k]);
          acc[2 * k + 1] = dot2(__builtin_amdgcn_perm(d[2 * t + 1][k], d[2 * t][k], 0x07060302u), pw[t], acc[2 * k + 1]);
        }
    }
#undef FETCH
    const f32x2 o2 = {rowsum4(acc[0], acc[2], acc[4], acc[6]), rowsum4(acc[1], acc[3], acc[5], acc[7])};
    *(LAS f32x2*)(L + L_OP + ci * 512 + (c * 8 + r * 2) * 4) = o2;
    wait_lgkm();
    __builtin_amdgcn_s_barrier();
    return;
  }

  const int w = wave - 4, kg = lane >> 4, c16 = lane & 15;
  const rsrc_t rs = __builtin_amdgcn_make_buffer_rsrc((void*)ws, 0, WS_END, 0x00020000);
  if (w == 0) STAMP(0);
  const bool mb = !side && g >= 4 && g < 12, f1 = !side && g >= 12 && g < 26, rr = (g & 7) == 1;
  const int jf = x * 7 + ((g - 12) >> 1), rbat = g >> 3;
  int ts = 0;
  auto tsync = [&](bool nap) { larrive(C_TS); ts += 4; lwait(C_TS, ts, nap); };
  auto tile_src = [&](const u16* pa, const u16* pb, int ld, int row0) -> const u16* {
    return ((lane & 4) ? pb : pa) + (lane & 3) * 8 + (size_t)(row0 + (lane >> 3)) * ld;
  };
  auto frag = [&](int buf, int t, int s) -> u32x4 {
    LAS unsigned char* p = L + buf + (s * 32 + kg * 8 + (c16 >> 2)) * 128 + (c16 & 2) * 32 + t * 16 + (c16 & 1) * 8;
    const u32x2 a = __builtin_bit_cast(u32x2, __builtin_amdgcn_ds_read_tr16_b64_v4i16((LAS s16x4*)p));
    const u32x2 b = __builtin_bit_cast(u32x2, __builtin_amdgcn_ds_read_tr16_b64_v4i16((LAS s16x4*)(p + 512)));
    return u32x4{a[0], a[1], b[0], b[1]};
  };
  auto pick = [&](const f32x4 (&a)[4]) -> float {
    const f32x4 v = kg == 0 ? a[0] : kg == 1 ? a[1] : kg == 2 ? a[2] : a[3];
    return rowsum4(v[0], v[1], v[2], v[3]);
  };
  auto lnorm = [&](u32x4 v, float (&n)[8]) {
#pragma unroll
    for (int k = 0; k < 4; ++k) { n[2 * k] = lo2f(v[k]); n[2 * k + 1] = hi2f(v[k]); }
    const float mean = wave_sum(((n[0] + n[1]) + (n[2] + n[3])) + ((n[4] + n[5]) + (n[6] + n[7]))) * (1.f / D);
    float q = 0.f;
#pragma unroll
    for (int k = 0; k < 8; ++k) { const float e = n[k] - mean; q = fmaf(e, e, q); }
    const float var = rb(wave_sum(q) * (1.f / D)), mr = rb(mean);
    const float inv = __builtin_amdgcn_rcpf(rb(__builtin_amdgcn_sqrtf(rb(var + 1e-5f))));
#pragma unroll
    for (int k = 0; k < 8; ++k) n[k] = rb(rb(n[k] - mr) * inv);
  };
  u32x4 W[16];
  auto modcols = [&](const u32x4 (&cv)[4]) -> float {
    f32x4 acc[4];
#pragma unroll
    for (int q = 0; q < 4; ++q) acc[q] = f32x4{0.f, 0.f, 0.f, 0.f};
#pragma unroll
    for (int t = 0; t < 4; ++t)
#pragma unroll
      for (int q = 0; q < 4; ++q) acc[q] = mfma(cv[t], W[4 * t + q], acc[q]);
    return rb(pick(acc));
  };
  auto load_c = [&](u32x4 (&cv)[4]) {
#pragma unroll
    for (int t = 0; t < 4; ++t) cv[t] = *(const GAS u32x4*)(cin + (lane & 3) * D + t * 128 + (c16 >> 2) * 32 + kg * 8);
  };
  auto silu_c = [&](u32x4 (&cv)[4]) {
#pragma unroll
    for (int t = 0; t < 4; ++t)
#pragma unroll
      for (int k = 0; k < 4; ++k) { const float p = lo2f(cv[t][k]), q = hi2f(cv[t][k]); cv[t][k] = pk2(p * sigmoidf_(p), q * sigmoidf_(q)); }
  };

  const int kq = g & 3, qb = (g >> 2) & 1;
  auto wq_tile = [&]() {
    const u16* p = tile_src(wq + head * DH + qb * 64, wq + head * DH + qb * 64 + 32, H * DH, kq * 128);
#pragma unroll
    for (int i = 0; i < 16; ++i) dma_at(p + (size_t)i * 8 * (H * DH), lbase + L_STG + i * 1024);
  };
  u32x2 ev = ld8(rs, WS_E);
#define TAGS() asm volatile("" : "+v"(ev)); const u32 tag0 = ev[0] + 1u, tag1 = ev[1] + 1u
  const u32 yoff = WS_Y + (bat * 64 + head * 2 + side) * 2048;
  if (w == 1) {
    const int band = x * 2 + (g & 1), sl = g >> 1;
    const u16* p = tile_src(wmod + band * 64, wmod + band * 64 + 32, 6 * D, sl * 32);
#pragma unroll
    for (int i = 0; i < 4; ++i) dma_at(p + (size_t)i * 8 * (6 * D), lbase + L_SC + i * 1024);
    u32x4 a = *(const GAS u32x4*)(cin + (c16 >> 2) * D + sl * 32 + kg * 8);
#pragma unroll
    for (int k = 0; k < 4; ++k) { const float p0 = lo2f(a[k]), p1 = hi2f(a[k]); a[k] = pk2(p0 * sigmoidf_(p0), p1 * sigmoidf_(p1)); }
    wait_vm<0>();
    TAGS();
    STAMP(6);
    const u32 po = WS_PG + (band * 16 + sl) * 2048 + (c16 >> 3) * 1024 + kg * 64 + (c16 & 7) * 8;
#pragma unroll
    for (int t = 0; t < 4; ++t) {
      const f32x4 acc = mfma(a, frag(L_SC, t, 0), f32x4{0.f, 0.f, 0.f, 0.f});
      st8(rs, po + t * 256, u32x2{__float_as_uint(acc[0]), (c16 & 1) ? tag1 : tag0});
    }
    STAMP(7);
  }
  if (w == 2 && (g & 3) == 3) {
    const int cg = x * 8 + (g >> 2);
    float sum = 0.f, sq = 0.f, xs[32];
#pragma unroll
    for (int i = 0; i < 4; ++i) {
      const u32x4 v = *(const GAS u32x4*)(xin + kg * D + i * 128 + c16 * 8);
#pragma unroll
      for (int k = 0; k < 4; ++k) { xs[8 * i + 2 * k] = lo2f(v[k]); xs[8 * i + 2 * k + 1] = hi2f(v[k]); }
    }
    const u32 xp = *(const GAS u32*)(xin + kg * D + cg * 8 + (c16 & 3) * 2);
#pragma unroll
    for (int i = 0; i < 32; ++i) sum += xs[i];
    const float mean = row_sum(sum) * (1.f / D);
#pragma unroll
    for (int i = 0; i < 32; ++i) { const float e = xs[i] - mean; sq = fmaf(e, e, sq); }
    const float var = rb(row_sum(sq) * (1.f / D)), mr = rb(mean);
    const float inv = __builtin_amdgcn_rcpf(rb(__builtin_amdgcn_sqrtf(rb(var + 1e-5f))));
    float n0 = rb(rb(lo2f(xp) - mr) * inv), n1 = rb(rb(hi2f(xp) - mr) * inv);
    asm volatile("" : "+v"(n0), "+v"(n1));
    TAGS();
    __builtin_amdgcn_s_sleep(ADLY);
    const u32 pi = WS_PG + ((cg >> 3) * 16 + (c16 >> 2)) * 2048 + (cg & 7) * 256 + kg * 64 + (c16 & 3) * 16;
    u32x4 v[8]; bool done[8]; int n = 0;
#pragma unroll
    for (int i = 0; i < 8; ++i) done[i] = false;
    for (;;) {
      CBAR();
      bool all = true;
#pragma unroll
      for (int i = 0; i < 8; ++i) if (!done[i]) v[i] = ld16(rs, pi + (i & 3) * 8192 + (i >> 2) * 262144);
#pragma unroll
      for (int i = 0; i < 8; ++i) { done[i] = v[i][1] == tag0 && v[i][3] == tag1; all &= done[i]; }
      if (__all(all) || gfail(n)) break;
      if (PSLP) __builtin_amdgcn_s_sleep(PSLP);
    }
    STAMP(5);
    float mv[4];
#pragma unroll
    for (int k = 0; k < 4; ++k) {
      const int i0 = (k >> 1) * 4, e = (k & 1) * 2;
      float t = (__uint_as_float(v[i0][e]) + __uint_as_float(v[i0 + 1][e])) + (__uint_as_float(v[i0 + 2][e]) + __uint_as_float(v[i0 + 3][e]));
      t += dpp<XOR8>(t); t += dpp<ROR4>(t);
      mv[k] = rb(t);
    }
    const u32 pr = pk2(rb(n0 * rb(1.f + mv[2])) + mv[0], rb(n1 * rb(1.f + mv[3])) + mv[1]);
    if (c16 < 4) st8(rs, WS_HG + cg * 128 + kg * 32 + c16 * 8, u32x2{pr, (c16 & 1) ? tag1 : tag0});
    STAMP(29);
  }
  if (w == 0) {
    wq_tile();
    wait_vm<0>();
    TAGS();
#pragma unroll
    for (int i = 0; i < 16; ++i) W[i] = frag(L_STG, i >> 2, i & 3);
#pragma unroll
    for (int i = 0; i < 16; ++i) asm volatile("" : "+v"(W[i]));
    STAMP(8);
    __builtin_amdgcn_s_sleep(HDLY);
    u32x4 v0, v1; int n = 0;
    const u32 ho = WS_HG + (kq * 16 + (c16 >> 2) * 4 + kg) * 128 + (lane & 3) * 32;
    bool hd = false;
    for (;;) {
      CBAR();
      if (!hd) { v0 = ld16(rs, ho); v1 = ld16(rs, ho + 16); }
      hd = v0[1] == tag0 && v0[3] == tag1 && v1[1] == tag0 && v1[3] == tag1;
      if (__all(hd) || gfail(n)) break;
      if (PSLP) __builtin_amdgcn_s_sleep(PSLP);
    }
    STAMP(9);
    const u32x4 a = {v0[0], v0[2], v1[0], v1[2]};
    float qp[4];
#pragma unroll
    for (int t = 0; t < 4; ++t) {
      f32x4 acc[4];
#pragma unroll
      for (int q = 0; q < 4; ++q) acc[q] = mfma(a, W[4 * t + q], f32x4{0.f, 0.f, 0.f, 0.f});
      qp[t] = pick(acc);
    }
    const u32 qo = WS_QG + (head * 4 + kg) * 4096 + (g & 7) * 512 + c16 * 16;
    st16(rs, qo, u32x4{__float_as_uint(qp[0]), tag0, __float_as_uint(qp[1]), tag1});
    st16(rs, qo + 256, u32x4{__float_as_uint(qp[2]), tag0, __float_as_uint(qp[3]), tag1});
    STAMP(28);
    if (QDLY) __builtin_amdgcn_s_sleep(QDLY);
    const u32 qi = WS_QG + (head * 4 + bat) * 4096 + (lane >> 5) * 2048 + (lane & 31) * 16;
    u32x4 v[4]; bool done[4] = {false, false, false, false};
    for (;;) {
      CBAR();
#pragma unroll
      for (int i = 0; i < 4; ++i) if (!done[i]) v[i] = ld16(rs, qi + i * 512);
#pragma unroll
      for (int i = 0; i < 4; ++i) done[i] = v[i][1] == tag0 && v[i][3] == tag1;
      if (__all(done[0] && done[1] && done[2] && done[3]) || gfail(n)) break;
      if (PSLP) __builtin_amdgcn_s_sleep(PSLP);
    }
    const int col = (lane >> 5) * 64 + (c16 >> 3) * 32 + (kg & 1) * 16 + (c16 & 7);
    *(LAS u16*)(L + L_Q + col * 2) = (u16)bfbits((__uint_as_float(v[0][0]) + __uint_as_float(v[1][0])) + (__uint_as_float(v[2][0]) + __uint_as_float(v[3][0])));
    *(LAS u16*)(L + L_Q + col * 2 + 16) = (u16)bfbits((__uint_as_float(v[0][2]) + __uint_as_float(v[1][2])) + (__uint_as_float(v[2][2]) + __uint_as_float(v[3][2])));
    wait_lgkm();
    ctl[C_QRDY] = 1;
  }
  TAGS();
  tsync(true);
  {
    const u32x4 sv = {SENT, SENT, SENT, SENT};
    if (w == 1) { st16(rs, yoff + lane * 16, sv); st16(rs, yoff + 1024 + lane * 16, sv); }
    if (w == 2) { if (lane == 0) st16(rs, WS_ML + blk * 128, sv);
      if (rr) st16(rs, WS_X1 + (lane >> 3) * 4096 + rbat * 1024 + x * 128 + (lane & 7) * 16, sv); }
    if (f1) { st16(rs, WS_Z + (w * 64 + jf) * 2048 + lane * 16, sv); st16(rs, WS_Z + (w * 64 + jf) * 2048 + 1024 + lane * 16, sv); }
  }
  const int mcol = 2 * D + (x * 4 + ((g - 4) >> 1)) * 64;
  if (mb || f1) {
    const int ld = f1 ? F : 6 * D;
    const u16* p = tile_src(f1 ? wg + jf * 32 : wmod + mcol, f1 ? wu + jf * 32 : wmod + mcol + 32, ld, w * 32);
#pragma unroll
    for (int t = 0; t < 4; ++t) {
      for (int i = 0; i < w; ++i) __builtin_amdgcn_s_sleep(SLP_BAND);
#pragma unroll
      for (int i = 0; i < 4; ++i) { dma_at(p + (size_t)(t * 128 + i * 8) * ld, lbase + L_STG + w * 4096 + i * 1024); __builtin_amdgcn_s_sleep(4 * SLP_BAND); }
      wait_vm<0>();
      tsync(true);
#pragma unroll
      for (int s = 0; s < 4; ++s) W[4 * t + s] = frag(L_STG, w, s);
      tsync(true);
#pragma unroll
      for (int s = 0; s < 4; ++s) asm volatile("" : "+v"(W[4 * t + s]));
    }
    if (mb) {
      u32x4 cv[4]; load_c(cv); silu_c(cv);
      const u32 mine = bfbits(modcols(cv)), pr = mine | (dppu<XOR1>(mine) << 16);
      if ((c16 & 1) == 0) st4(rs, WS_MOD + (kg * 6 * D + mcol + (c16 >> 3) * 32 + w * 8 + (c16 & 7)) * 2, pr);
    }
  }
  wait_vm<0>();
  tsync(true);
  if (w == 0) { if (lane == 0) st8(rs, WS_FLAG + blk * 8, u32x2{tag0, tag1}); STAMP(10); }
  lwait(C_KCNT, 3, true);
  const float m = fmaxf(fmaxf(ctf[C_KMAX], ctf[C_KMAX + 1]), ctf[C_KMAX + 2]);
  {
    float l = 0.f;
#pragma unroll
    for (int i = 0; i < 2; ++i) {
      LAS u32x4* p = (LAS u32x4*)(L + L_SC + (i * 256 + w * 64 + lane) * 16);
      u32x4 v = *p;
#pragma unroll
      for (int k = 0; k < 4; ++k) {
        const float a = __builtin_amdgcn_exp2f((lo2f(v[k]) - m) * 1.4426950408889634f), b = __builtin_amdgcn_exp2f((hi2f(v[k]) - m) * 1.4426950408889634f);
        l += a + b; v[k] = pk2(a, b);
      }
      *p = v;
    }
    ctf[C_L + w] = wave_sum(l);
    larrive(C_PCNT);
  }
  float lsum = 0.f;
  if (w == 0) {
    lwait(C_PCNT, 4, false);
    lsum = (ctf[C_L] + ctf[C_L + 1]) + (ctf[C_L + 2] + ctf[C_L + 3]);
    if (lane == 0) st8(rs, WS_ML + blk * 128, u32x2{clean(__float_as_uint(m)), clean(__float_as_uint(lsum))});
  }
  u32x4 B[4][8], t0[8];
  auto wperm = [&](int s) {
#pragma unroll
    for (int j = 0; j < 8; ++j)
#pragma unroll
      for (int k = 0; k < 4; ++k) B[s][j][k] = __builtin_amdgcn_perm(t0[2 * k + 1][j >> 1], t0[2 * k][j >> 1], (j & 1) ? 0x07060302u : 0x05040100u);
#pragma unroll
    for (int j = 0; j < 8; ++j) asm volatile("" : "+v"(B[s][j]));
  };
  {
    const u16* p = wo + (size_t)(head * DH + kg * 8) * D + w * 128 + c16 * 8;
    for (int i = 0; i < w; ++i) __builtin_amdgcn_s_sleep(SLP_WO);
#pragma unroll
    for (int s = 0; s < 4; ++s) {
#pragma unroll
      for (int j = 0; j < 8; ++j) { CBAR(); t0[j] = *(const GAS u32x4*)(p + (size_t)(s * 32 + j) * D); CBAR(); __builtin_amdgcn_s_sleep(4 * SLP_WO); }
      wperm(s);
    }
  }
  if (w == 0) {
    int n = 0;
    for (;;) {
      CBAR();
      const u32x4 a = ld16(rs, WS_FLAG + lane * 32), b = ld16(rs, WS_FLAG + lane * 32 + 16);
      const bool ok = a[0] == tag0 && a[1] == tag1 && a[2] == tag0 && a[3] == tag1 && b[0] == tag0 && b[1] == tag1 && b[2] == tag0 && b[3] == tag1;
      if (__all(ok) || gfail(n)) break;
      __builtin_amdgcn_s_sleep(32);
    }
    ctl[C_INIT] = 1;
    if (blk == 0 && lane == 0) st8(rs, WS_E, u32x2{tag0, tag1});
    u32x2 o;
    for (;;) {
      CBAR();
      o = ld8(rs, WS_ML + (blk ^ 8) * 128);
      if (__all(o[0] != SENT && o[1] != SENT) || gfail(n)) break;
      __builtin_amdgcn_s_sleep(32);
    }
    const float m2 = __uint_as_float(o[0]), l2 = __uint_as_float(o[1]), M = fmaxf(m, m2);
    const float e1 = __builtin_amdgcn_exp2f((m - M) * 1.4426950408889634f), e2 = __builtin_amdgcn_exp2f((m2 - M) * 1.4426950408889634f);
    ctf[C_SCALE] = e1 / (lsum * e1 + l2 * e2);
    STAMP(11);
  }
  lwait(C_INIT, 1, true);
  u32x4 ma = {0u, 0u, 0u, 0u}, mc = ma;
  if (f1) { ma = ld16(rs, WS_MOD + (w * 6 * D + 3 * D + lane * 8) * 2); mc = ld16(rs, WS_MOD + (w * 6 * D + 4 * D + lane * 8) * 2); }
  if (rr) { ma[0] = ld4(rs, WS_MOD + (rbat * 6 * D + 2 * D + x * 64 + (lane & ~1)) * 2); ma[1] = ld4(rs, WS_MOD + (rbat * 6 * D + 5 * D + x * 64 + (lane & ~1)) * 2);
            mc[0] = *(const GAS u16*)(xin + rbat * D + x * 64 + lane); }
  asm volatile("s_waitcnt vmcnt(0) lgkmcnt(0)" : "+v"(ma), "+v"(mc) :: "memory");
  __builtin_amdgcn_s_barrier();
  CBAR();
  if (w == 0) STAMP(12);
  {
    const float sc = ctf[C_SCALE];
    const LAS f32x2* op = (const LAS f32x2*)(L + L_OP) + lane;
    const f32x2 o = ((op[0] + op[64]) + op[128]) * sc;
    const u32 hi = pk2(o[0], o[1]), lo = pk2(o[0] - lo2f(hi), o[1] - hi2f(hi));
    *(LAS u32*)(L + L_AROW + w * 512 + lane * 4) = hi;
    *(LAS u32*)(L + L_AROW + w * 512 + 256 + lane * 4) = lo;
    f32x4 acc[8];
#pragma unroll
    for (int j = 0; j < 8; ++j) acc[j] = f32x4{0.f, 0.f, 0.f, 0.f};
#pragma unroll
    for (int s = 0; s < 4; ++s) {
      const u32x4 a = *(LAS u32x4*)(L + L_AROW + w * 512 + (lane & 1) * 256 + s * 64 + kg * 16);
#pragma unroll
      for (int j = 0; j < 8; ++j) acc[j] = mfma(a, B[s][j], acc[j]);
    }
    u32x4 y;
#pragma unroll
    for (int k = 0; k < 4; ++k) y[k] = clean(__float_as_uint(kg == 0 ? acc[k][0] + acc[k][1] : acc[k + 4][0] + acc[k + 4][1]));
    if (kg < 2) st16(rs, yoff + (w * 128 + c16 * 8 + kg * 4) * 4, y);
  }
  if (w == 0) STAMP(13);
  if (rr) {
    float base = lo2f(mc[0]);
    int n = 0;
#pragma nounroll
    for (int pass = 0; pass < 2; ++pass) {
      const u32 off = (pass ? WS_Z : WS_Y) + (rbat * 64 + w * 16 + kg) * 2048 + (x * 64 + c16 * 4) * 4;
      const bool cut = pass && w == 3;
      u32x4 v[4]; bool done[4] = {false, false, cut, cut};
      v[2] = v[3] = u32x4{0u, 0u, 0u, 0u};
      for (;;) {
        CBAR();
#pragma unroll
        for (int i = 0; i < 4; ++i) if (!done[i]) v[i] = ld16(rs, off + i * 8192);
#pragma unroll
        for (int i = 0; i < 4; ++i) done[i] = ok4(v[i]);
        if (__all(done[0] && done[1] && done[2] && done[3]) || gfail(n)) break;
      }
      if (w == 0 && pass) STAMP(18);
      const f32x4 s = (__builtin_bit_cast(f32x4, v[0]) + __builtin_bit_cast(f32x4, v[1])) + (__builtin_bit_cast(f32x4, v[2]) + __builtin_bit_cast(f32x4, v[3]));
      *(LAS float*)(L + L_PART + (w * 64 + c16 * 4 + kg) * 4) = rowsum4(s[0], s[1], s[2], s[3]);
      wait_lgkm();
      __builtin_amdgcn_s_barrier();
      CBAR();
      if (w == 0) {
        const LAS float* pp = (const LAS float*)(L + L_PART) + lane;
        const float a = rb((pp[0] + pp[64]) + (pp[128] + pp[192]));
        const u32 gw = pass ? ma[1] : ma[0];
        const u32 mine = bfbits(base + rb(((lane & 1) ? hi2f(gw) : lo2f(gw)) * a));
        base = lo2f(mine);
        const u32 pr0 = mine | (dppu<XOR1>(mine) << 16);
        if (pass) { if ((lane & 1) == 0) *(GAS u32*)(out + rbat * D + x * 64 + lane) = pr0; STAMP(19); }
        else {
          const u32 pr = clean(pr0);
          const u32x4 oa = {dppu<BCAST + 0>(pr), dppu<BCAST + 2>(pr), dppu<BCAST + 4>(pr), dppu<BCAST + 6>(pr)};
          const u32x4 ob = {dppu<BCAST + 8>(pr), dppu<BCAST + 10>(pr), dppu<BCAST + 12>(pr), dppu<BCAST + 14>(pr)};
          st16(rs, WS_X1 + (lane & 7) * 4096 + rbat * 1024 + x * 128 + (lane >> 3) * 16, (lane & 8) ? ob : oa);
          STAMP(14);
        }
      }
      if (pass == 0) {
        for (;;) {
          CBAR();
          const u32 t = ld4(rs, WS_X1 + x * 4096 + kg * 1024 + x * 128);
          if (__all(t != SENT) || gfail(n)) break;
        }
        if (w == 0) STAMP(17);
        __builtin_amdgcn_s_sleep(ZDLY);
      }
    }
  } else if (f1) {
    {
      const u16* p = wd + (size_t)(jf * 32 + kg * 8) * D + w * 128 + c16 * 8;
#pragma unroll
      for (int j = 0; j < 8; ++j) t0[j] = *(const GAS u32x4*)(p + (size_t)j * D);
      wperm(0);
    }
    u32x4 v; bool done = false; int n = 0;
    for (;;) {
      CBAR();
      if (!done) v = ld16(rs, WS_X1 + x * 4096 + w * 1024 + lane * 16);
      done = ok4(v);
      if (__all(done) || gfail(n)) break;
    }
    float nn[8]; lnorm(v, nn);
    u32x4 h2;
#pragma unroll
    for (int k = 0; k < 4; ++k) h2[k] = pk2(rb(nn[2 * k] * rb(1.f + lo2f(mc[k]))) + lo2f(ma[k]), rb(nn[2 * k + 1] * rb(1.f + hi2f(mc[k]))) + hi2f(ma[k]));
    *(LAS u32x4*)(L + L_STG + w * 1040 + lane * 16) = h2;
    wait_lgkm();
    __builtin_amdgcn_s_barrier();
    CBAR();
    if (w == 0) STAMP(15);
    u32x4 a4[4];
#pragma unroll
    for (int t = 0; t < 4; ++t) a4[t] = *(LAS u32x4*)(L + L_STG + (lane & 3) * 1040 + (t * 128 + (c16 >> 2) * 32 + kg * 8) * 2);
    const float gv = modcols(a4), uv = dpp<XOR8>(gv);
    const float ge = rb(gv * sigmoidf_(1.5957691216057308f * (gv + 0.044715f * gv * gv * gv)));
    if (c16 < 8) *(LAS u16*)(L + L_PART + kg * 64 + (w * 8 + c16) * 2) = (u16)bfbits(ge * uv);
    wait_lgkm();
    __builtin_amdgcn_s_barrier();
    CBAR();
    const u32x4 a = *(LAS u32x4*)(L + L_PART + ((c16 & 1) * 2 + (c16 >> 3)) * 64 + kg * 16);
    f32x4 acc[8];
#pragma unroll
    for (int j = 0; j < 8; ++j) acc[j] = mfma(a, B[0][j], f32x4{0.f, 0.f, 0.f, 0.f});
#pragma unroll
    for (int i = 0; i < 2; ++i) {
      u32x4 z;
#pragma unroll
      for (int k = 0; k < 4; ++k) z[k] = clean(__float_as_uint((kg & 1) ? acc[k + 4][i] : acc[k][i]));
      st16(rs, WS_Z + ((2 * i + (kg >> 1)) * 64 + jf) * 2048 + (w * 128 + c16 * 8 + (kg & 1) * 4) * 4, z);
    }
    if (w == 0) STAMP(16);
  }
  if (w == 0) STAMP_OUT();
}

extern "C" void kernel_launch(void* const* d_in, const int* in_sizes, int n_in, void* d_out, int out_size, void* d_ws, size_t ws_size,
                              hipStream_t stream) {
  adaln_block_kernel<<<256, 512, 0, stream>>>((const u16*)d_in[0], (const u16*)d_in[1], (const u16*)d_in[2], (const u16*)d_in[3], (const u16*)d_in[4],
      (const u16*)d_in[6], (const u16*)d_in[7], (const u16*)d_in[8], (const u16*)d_in[9], (const u16*)d_in[10], (u16*)d_out, (unsigned char*)d_ws);
}
```

```cpp
#include <hip/hip_runtime.h>
#include <hip/hip_bf16.h>
#include <cstdint>

typedef unsigned short u16;
typedef unsigned u32;
typedef unsigned long long u64;
using u32x2 = __attribute__((ext_vector_type(2))) unsigned;
using u32x4 = __attribute__((ext_vector_type(4))) unsigned;
using f32x2 = __attribute__((ext_vector_type(2))) float;
using f32x4 = __attribute__((ext_vector_type(4))) float;
using bf16x2 = __attribute__((ext_vector_type(2))) __bf16;
using bf16x8 = __attribute__((ext_vector_type(8))) __bf16;
using u64x4 = __attribute__((ext_vector_type(4))) unsigned long long;
using u64x8 = __attribute__((ext_vector_type(8))) unsigned long long;
using s16x4 = __attribute__((ext_vector_type(4))) short;
typedef __amdgpu_buffer_rsrc_t rsrc_t;
#define LAS __attribute__((address_space(3)))
#define GAS __attribute__((address_space(1)))
#define DEV __device__ __forceinline__
#define INL __attribute__((always_inline))

constexpr int WIN = 19;
constexpr int WFR = -64;
constexpr int WLO = 4;
constexpr int WHI = 6;
constexpr int PWIN = 22;
constexpr int PGAIN = 14;
constexpr int SLOPE = 432;
constexpr int SB0 = 184;
constexpr int HTIME = 690;
constexpr int QTIME = 935;
constexpr int OTIME = 400;
constexpr int ZDLY = 12;
constexpr int SLP_WO = 3;
constexpr int NPRE = 7;
constexpr int MODLATE = 255;
constexpr int DOZE = 32;
constexpr int NAP = 1;
constexpr int CTIME = 230;
constexpr int CAPSH = 23;

constexpr int D = 512, H = 32, SK = 8192, DH = 128, F = 1792;
constexpr int NS = 16, UNIT = 8192, NU = 128;
constexpr int GCAP = 1 << CAPSH, LCAP = 4 << CAPSH;

constexpr u32 WS_E = 0, WS_NEAR = 4096, WS_PROBE = 16384, WS_MOD = 32768, WS_ML = 65536, WS_X1 = 131072, WS_HG = 196608, WS_PC = 262144,
              WS_Q = 393216, WS_Y = 524288, WS_Z = 1048576, WS_END = 1572864;
constexpr int L_SC = NS * UNIT, L_STG = L_SC + 8192, L_QP = L_STG + 4352, L_Q = L_QP + 4160, L_OP = L_Q + 256,
              L_PART = L_OP + 1024, L_NV = L_PART + 1024, L_CTL = L_NV + 512, L_END = L_CTL + 512;
static_assert(L_END <= 163840 && L_END > 81920, "one workgroup per CU");
static_assert(3 * (NPRE - 1) > 16, "pace() starts at unit 32");
enum { C_LANDED = 0, C_ABORT, C_QRDY, C_KCNT, C_PCNT, C_TS, C_INIT, C_SCALE, C_KMAX = 8, C_NEAR = 11, C_L = 12, C_TAG = 16, C_DONE = 20, C_GATE = 23, C_SCH = 24, C_MEAN = 28, C_CPT = 48 };

DEV float lo2f(u32 v) { return __builtin_bit_cast(float, v << 16); }
DEV float hi2f(u32 v) { return __builtin_bit_cast(float, v & 0xffff0000u); }
DEV u32 pk2(float a, float b) { bf16x2 r = {(__bf16)a, (__bf16)b}; return __builtin_bit_cast(u32, r); }
DEV u32 bfbits(float a) { return (u32)__builtin_bit_cast(u16, (__bf16)a); }
DEV float rb(float a) { return lo2f(bfbits(a)); }
DEV float dot2(u32 a, u32 b, float c) { return __builtin_amdgcn_fdot2_f32_bf16(__builtin_bit_cast(bf16x2, a), __builtin_bit_cast(bf16x2, b), c, false); }
template <int CTRL> DEV u32 dppu(u32 x) { return (u32)__builtin_amdgcn_mov_dpp((int)x, CTRL, 0xf, 0xf, true); }
template <int CTRL> DEV float dpp(float x) { return __builtin_bit_cast(float, dppu<CTRL>(__builtin_bit_cast(u32, x))); }
constexpr int XOR1 = 0xB1, XOR2 = 0x4E, XOR7 = 0x141, XOR8 = 0x128;
DEV float tsum32(float a, float b) { auto s = __builtin_amdgcn_permlane32_swap(__float_as_uint(a), __float_as_uint(b), false, false); return __uint_as_float(s[0]) + __uint_as_float(s[1]); }
DEV float tsum16(float a, float b) { auto s = __builtin_amdgcn_permlane16_swap(__float_as_uint(a), __float_as_uint(b), false, false); return __uint_as_float(s[0]) + __uint_as_float(s[1]); }
DEV float row_sum(float x) { x += dpp<XOR1>(x); x += dpp<XOR2>(x); x += dpp<XOR7>(x); return x + dpp<XOR8>(x); }
DEV float wave_sum(float x) { x = row_sum(x); x = tsum16(x, x); return tsum32(x, x); }
DEV float wave_max(float x) {
  x = fmaxf(x, dpp<XOR1>(x)); x = fmaxf(x, dpp<XOR2>(x)); x = fmaxf(x, dpp<XOR7>(x)); x = fmaxf(x, dpp<XOR8>(x));
  auto s = __builtin_amdgcn_permlane16_swap(__float_as_uint(x), __float_as_uint(x), false, false); x = fmaxf(__uint_as_float(s[0]), __uint_as_float(s[1]));
  auto t = __builtin_amdgcn_permlane32_swap(__float_as_uint(x), __float_as_uint(x), false, false); return fmaxf(__uint_as_float(t[0]), __uint_as_float(t[1]));
}
DEV float rowsum4(float v0, float v1, float v2, float v3) { return tsum16(tsum32(v0, v2), tsum32(v1, v3)); }
DEV f32x4 mfma(u32x4 a, u32x4 b, f32x4 c) { return __builtin_amdgcn_mfma_f32_16x16x32_bf16(__builtin_bit_cast(bf16x8, a), __builtin_bit_cast(bf16x8, b), c, 0, 0, 0); }
DEV float sigmoidf_(float v) { return __builtin_amdgcn_rcpf(1.f + __builtin_amdgcn_exp2f(-1.4426950408889634f * v)); }

#define CBAR() asm volatile("" ::: "memory")
#define SCHED() __builtin_amdgcn_sched_barrier(0)
template <int N> DEV void wait_vm() { asm volatile("s_waitcnt vmcnt(%0)" :: "i"(N) : "memory"); }
DEV void wait_lgkm() { asm volatile("s_waitcnt lgkmcnt(0)" ::: "memory"); }
DEV u32x4 ld16(rsrc_t r, u32 off) { return __builtin_bit_cast(u32x4, __builtin_amdgcn_raw_buffer_load_b128(r, off, 0, 16)); }
DEV u32x2 ld8(rsrc_t r, u32 off) { return __builtin_bit_cast(u32x2, __builtin_amdgcn_raw_buffer_load_b64(r, off, 0, 16)); }
DEV void st16(rsrc_t r, u32 off, u32x4 v) { __builtin_amdgcn_raw_buffer_store_b128(v, r, off, 0, 16); }
DEV void st8(rsrc_t r, u32 off, u32x2 v) { __builtin_amdgcn_raw_buffer_store_b64(v, r, off, 0, 16); }

DEV u32 or4(u32x4 v) { return (v[0] | v[1]) | (v[2] | v[3]); }
DEV u32 tag12(float v, u32 t) { u32 r; asm("v_bfi_b32 %0, %1, %2, %3" : "=v"(r) : "s"(0xfffu), "v"(t), "v"(v)); return r; }
template <int N> DEV void poll16(rsrc_t r, u32x4 (&v)[N], const u32 (&o)[N], u32x4 t) {
  for (int n = GCAP;;) {
    CBAR();
    u32 bad = 0;
#pragma unroll
    for (int i = 0; i < N; ++i) v[i] = ld16(r, o[i]);
    u32 msk = --n > 0 ? 0xffffu : 0u;
    asm volatile("" : "+s"(msk));
#pragma unroll
    for (int i = 0; i < N; ++i) { v[i] ^= t; bad |= or4(v[i]); }
    if (__builtin_amdgcn_ballot_w64((bad & msk) != 0u) == 0) break;
  }
}

DEV void set_m0(u32 v) { asm volatile("s_mov_b32 m0, %0\n\ts_nop 0" :: "s"(v) : "memory"); }
DEV void dma(u32 voff, const void* base) { asm volatile("global_load_lds_dwordx4 %0, %1" :: "v"(voff), "s"(base) : "memory"); }
DEV void dma_at_nt(const void* p, u32 m0v) { asm volatile("s_mov_b32 m0, %1\n\ts_nop 0\n\tglobal_load_lds_dwordx4 %0, off nt" :: "v"(p), "s"(m0v) : "memory"); }
DEV void dma_at(const void* p, u32 m0v) { asm volatile("s_mov_b32 m0, %1\n\ts_nop 0\n\tglobal_load_lds_dwordx4 %0, off" :: "v"(p), "s"(m0v) : "memory"); }

constexpr int NV = WLO + WHI + 1, VSZ = 180;
DEV void stream_kv(int wave, const char* kb, const char* vb, u32 voff, u32 actl, u32 lbase) {
  u32 vo, vu, vp, su, sc, sa, sb; u64 base;
  asm volatile(
      "s_cmp_lg_u32 %[wave], 0\n\t"
      "s_cbranch_scc1 .Lskip%=\n\t"
      "s_add_u32 m0, %[lb], 0x4000\n\t"
      "s_mov_b64 %[base], %[kb]\n\t"
      "v_add_u32 %[vo], 0x4000, %[voff]\n\t"
      "v_mov_b32 %[vu], 2\n\t"
      "s_mov_b32 %[su], 0\n\t"
      "s_mov_b32 %[sb], 0\n\t"
      "s_mov_b32 %[sc], 1\n\t"
      "s_getpc_b64 s[40:41]\n"
      ".Ltbl%=:\n\t"
      ".set wv_, %c[win]\n\t"
      ".rept %c[nv]\n"
      "1:\n\t"
      ".set pi_, 0\n\t"
      ".rept 8\n\t"
      "global_load_lds_dwordx4 %[vo], %[base] offset:(pi_&3)*1024 nt\n\t"
      ".if (pi_&3) == 3\n\t"
      "s_add_u32 m0, m0, 0x1000\n\t"
      "v_add_u32 %[vo], 0x1000, %[vo]\n\t"
      ".endif\n\t"
      ".if pi_ == 5\n\t"
      "ds_read_b128 v[20:23], %[actl] offset:%c[odone]\n\t"
      "ds_read_b64 v[24:25], %[actl] offset:%c[osch]\n\t"
      "s_memrealtime s[44:45]\n\t"
      ".endif\n\t"
      ".if pi_ == ((wv_-1)&7)\n\t"
      "v_subrev_u32 %[vp], (wv_-1)>>3, %[vu]\n\t"
      ".endif\n\t"
      "s_waitcnt vmcnt(wv_)\n\t"
      ".if pi_ == ((wv_-1)&7)\n\t"
      "ds_write_b32 %[actl], %[vp]\n\t"
      ".endif\n\t"
      ".set pi_, pi_+1\n\t"
      ".endr\n\t"
      "v_add_u32 %[vu], 1, %[vu]\n\t"
      "s_sub_u32 %[sc], %[sc], 1\n\t"
      "s_cbranch_scc0 1b\n\t"
      "s_branch .Lnext%=\n\t"
      ".set wv_, wv_+1\n\t"
      ".if wv_ > %c[win]+%c[whi]\n\t"
      ".set wv_, %c[win]-%c[wlo]\n\t"
      ".endif\n\t"
      ".endr\n"
      ".Lnext%=:\n\t"
      ".if .Lnext%=-.Ltbl%= != %c[nv]*%c[vsz]\n\t"
      ".error \"loop body size\"\n\t"
      ".endif\n\t"
      "s_add_u32 %[su], %[su], 4\n\t"
      "s_add_u32 %[sa], %[su], 12\n\t"
      "s_lshr_b32 %[sa], %[sa], 4\n\t"
      "v_lshl_add_u32 %[vp], %[sa], 2, %[actl]\n\t"
      "s_cmp_eq_u32 %[su], 128\n\t"
      "s_cselect_b64 %[base], %[vb], %[base]\n\t"
      "v_add_u32 %[vo], 0x8000, %[vo]\n\t"
      "v_and_b32 %[vo], 0x1fffff, %[vo]\n\t"
      "s_and_b32 %[sa], %[su], 15\n\t"
      "s_lshl_b32 %[sa], %[sa], 13\n\t"
      "s_add_u32 m0, %[sa], %[lb]\n\t"
      "s_waitcnt lgkmcnt(0)\n\t"
      "v_mov_b32 %[vu], s44\n\t"
      "ds_write_b32 %[vp], %[vu] offset:%c[ocpt]\n\t"
      "s_cmp_eq_u32 %[su], 256\n\t"
      "s_cbranch_scc1 .Lend%=\n\t"
      "v_readfirstlane_b32 %[sc], v25\n\t"
      "v_readfirstlane_b32 %[sa], v24\n\t"
      "v_mov_b32 %[vu], %[su]\n\t"
      "s_mul_i32 s42, %[sc], %[su]\n\t"
      "s_lshr_b32 s42, s42, 4\n\t"
      "s_sub_u32 %[sa], s44, %[sa]\n\t"
      "s_sub_u32 %[sa], %[sa], s42\n\t"
      "s_mul_i32 %[sa], %[sa], %c[gain]\n\t"
      "s_add_u32 %[sa], %[sa], %c[rnd]\n\t"
      "s_ashr_i32 %[sa], %[sa], 8\n\t"
      "s_min_i32 %[sa], %[sa], %c[whi]\n\t"
      "s_max_i32 %[sa], %[sa], -%c[wlo]\n\t"
      "s_add_u32 %[sc], %[sa], %c[nv]\n\t"
      "s_min_u32 %[sc], %[sc], %[sa]\n\t"
      "s_mul_i32 %[sc], %[sc], %c[vsz]\n\t"
      "s_add_u32 s42, s40, %[sc]\n\t"
      "s_addc_u32 s43, s41, 0\n\t"
      "s_mov_b32 %[sc], 3\n"
      ".Lchk%=:\n\t"
      "v_min3_i32 %[vp], v20, v21, v22\n\t"
      "v_min_i32 %[vp], %[vp], v23\n\t"
      "s_nop 0\n\t"
      "v_readfirstlane_b32 %[sa], %[vp]\n\t"
      "s_add_u32 %[sa], %[sa], %c[ns]-4\n\t"
      "s_cmp_lt_i32 %[sa], %[su]\n\t"
      "s_cbranch_scc1 .Lfull%=\n\t"
      "s_setpc_b64 s[42:43]\n"
      ".Lfull%=:\n\t"
      "ds_read_b128 v[20:23], %[actl] offset:%c[odone]\n\t"
      "s_add_u32 %[sb], %[sb], 1\n\t"
      "s_cmp_lt_u32 %[sb], %[cap]\n\t"
      "s_waitcnt lgkmcnt(0)\n\t"
      "s_cbranch_scc1 .Lchk%=\n\t"
      "s_setpc_b64 s[42:43]\n"
      ".Lend%=:\n\t"
      ".set pi_, 3\n\t"
      ".rept 4\n\t"
      "v_mov_b32 %[vp], 256-pi_\n\t"
      "s_waitcnt vmcnt(pi_*8)\n\t"
      "ds_write_b32 %[actl], %[vp]\n\t"
      ".set pi_, pi_-1\n\t"
      ".endr\n\t"
      "s_waitcnt lgkmcnt(0)\n\t"
      "s_endpgm\n"
      ".Lskip%=:"
      : [vo] "=&v"(vo), [vu] "=&v"(vu), [vp] "=&v"(vp), [su] "=&s"(su), [sc] "=&s"(sc), [sa] "=&s"(sa), [sb] "=&s"(sb), [base] "=&s"(base)
      : [wave] "s"(wave), [kb] "s"(kb), [vb] "s"(vb), [voff] "v"(voff), [actl] "v"(actl), [lb] "s"(lbase), [cap] "s"(LCAP),
        [win] "i"(WIN), [whi] "i"(WHI), [wlo] "i"(WLO), [nv] "i"(NV), [vsz] "i"(VSZ), [ns] "i"(NS), [odone] "i"(C_DONE * 4), [ocpt] "i"(C_CPT * 4),
        [osch] "i"(C_SCH * 4), [gain] "i"(256 / PGAIN), [rnd] "i"(128 + WFR)
      : "memory", "scc", "v20", "v21", "v22", "v23", "v24", "v25", "s40", "s41", "s42", "s43", "s44", "s45");
}

__global__ void __launch_bounds__(512, 2) adaln_block_kernel(const u16*, const u16*, const u16*, const u16*, const u16*, const u16*, const u16*, const u16*,
                                                             const u16*, const u16*, u16*, unsigned char*) {
  u64x8 ka; u64x4 kb;
  asm volatile("s_load_dwordx16 %0, %2, 0x0\n\ts_load_dwordx8 %1, %2, 0x40" : "=&s"(ka), "=&s"(kb) : "s"(__builtin_amdgcn_kernarg_segment_ptr()));
  __shared__ __attribute__((aligned(16))) unsigned char lds_raw[L_END];
  LAS unsigned char* L = (LAS unsigned char*)lds_raw;
  volatile LAS int* ctl = (volatile LAS int*)(L + L_CTL);
  volatile LAS float* ctf = (volatile LAS float*)(L + L_CTL);
  const u32 lbase = (u32)(size_t)L;
  const int tid = threadIdx.x, lane = tid & 63, wave = __builtin_amdgcn_readfirstlane(tid >> 6);
  const int blk = blockIdx.x, x = blk & 7, g = blk >> 3;
  const int head = (g >> 3) * 8 + x, bat = (g >> 1) & 3, side = g & 1;
  if (wave == 3) {
    u64 pc; int len;
    asm volatile("s_getpc_b64 %0\n.Lkpc:\n\ts_mov_b32 %1, .Lkend-.Lkpc" : "=s"(pc), "=s"(len));
    const int o = (lane * 32 + g) * 64;
    if (o < len) asm volatile("s_mov_b32 m0, %2\n\ts_nop 0\n\tglobal_load_lds_dword %0, %1" :: "v"(o), "s"(pc), "s"(lbase + L_OP) : "memory");
  }
  u64 cbase = ((size_t)(bat * H + head) * SK + (size_t)side * 128) * DH * 2;
  asm volatile("s_waitcnt lgkmcnt(0)" : "+s"(ka), "+s"(kb), "+s"(cbase) :: "memory");
  const u16 *xin = (const u16*)ka[0], *kc = (const u16*)ka[1], *vc = (const u16*)ka[2], *wq = (const u16*)ka[3], *wo = (const u16*)ka[4],
            *wg = (const u16*)ka[5], *wu = (const u16*)ka[6], *wd = (const u16*)ka[7], *cin = (const u16*)kb[0], *wmod = (const u16*)kb[1];
  u16* out = (u16*)kb[2];
  unsigned char* ws = (unsigned char*)kb[3];
  if (wave == 0) {
    __builtin_amdgcn_s_setprio(3);
#pragma unroll
    for (int i = 0; i < 4; ++i) {
      set_m0(lbase + i * 4096);
      asm volatile("global_load_lds_dwordx4 %0, %1 nt\n\tglobal_load_lds_dwordx4 %0, %1 offset:1024 nt\n\tglobal_load_lds_dwordx4 %0, %1 offset:2048 nt\n\t"
                   "global_load_lds_dwordx4 %0, %1 offset:3072 nt" :: "v"(lane * 16 + i * 4096), "s"((const char*)kc + cbase) : "memory");
    }
  }
  const u32 gb = 1u << g;
  const bool hp = gb & 0x5u, mb = gb & 0x550u, f1 = gb & 0x1555000u, qp = gb & 0x88888888u, tile = gb & 0x89dddddd;
  const u32 tstart = (u32)__builtin_amdgcn_s_memrealtime();
  if (wave == 1) {
    ctl[lane] = (lane == C_GATE) * (tile ? 3 - NS / 2 : 2 * NU) + (lane == C_SCH) * ((int)tstart + SB0) + (lane == C_SCH + 1) * SLOPE;
  }
  asm volatile("s_waitcnt lgkmcnt(0)\n\ts_barrier" ::: "memory");
  const rsrc_t rs = __builtin_amdgcn_make_buffer_rsrc((void*)ws, 0, WS_END, 0x00020000);
  if (wave == 1) {
    const u32x2 ev = ld8(rs, WS_E), tv = ((ev & 0x7f7f7f7fu) + 0x01010101u) ^ (ev & 0x80808080u);
    if ((g & 15) == 13 && lane == 0) st8(rs, WS_PC + ((g >> 4) * 8 + x) * 8, u32x2{tstart, tv[0]});
    ctl[C_TAG] = (int)tv[0]; ctl[C_TAG + 1] = (int)tv[1]; ctl[C_TAG + 2] = 1;
  }
  const int jf = x * 7 + ((g - 12) >> 1);
  const int ca = hp ? x * 64 + g * 16 : f1 ? jf * 32 : qp ? head * DH + (g & 4) * 16 : mb ? 2 * D - 128 + x * 256 + g * 32 : x * 64;
  auto lwait = [&](int idx, int want, bool nap) INL {
    int n = 0;
    while (ctl[idx] < want) {
      if (nap) __builtin_amdgcn_s_sleep(1);
      if ((++n & 255) == 0) { if (ctl[C_ABORT]) break; if (n > LCAP) { ctl[C_ABORT] = 1; break; } }
    }
    CBAR();
  };
  auto doze = [&](int u) INL { for (int n = LCAP >> 5; n > 0 && ctl[C_LANDED] < u; --n) __builtin_amdgcn_s_sleep(DOZE); };
  auto larrive = [&](int idx) INL { wait_lgkm(); if (lane == 0) __hip_atomic_fetch_add((LAS int*)(L + L_CTL) + idx, 1, __ATOMIC_RELAXED, __HIP_MEMORY_SCOPE_WORKGROUP); };

  if (wave == 0) {
    const int ld = f1 ? F : qp ? H * DH : 6 * D, wi = side ? g >> 2 : (g - 10) >> 1;
    u32 dst = lbase + NS / 2 * UNIT;
    if (tile) {
      const u16* pa = (f1 ? wg : qp ? wq : wmod) + ca;
      const u16* p = ((lane & 4) ? (f1 ? wu + ca : pa + (hp ? D : 32)) : pa) + (lane & 3) * 8 + (lane >> 3) * ld;
      if (qp || mb) {
#pragma nounroll
        for (int i = 0; i < 64; ++i) { dma_at_nt(p, dst); wait_vm<PWIN>(); p += 8 * ld; dst += 1024; }
      } else {
#pragma nounroll
        for (int i = 0; i < 64; ++i) { dma_at(p, dst); wait_vm<PWIN>(); p += 8 * ld; dst += 1024; }
      }
    } else {
      set_m0(dst);
      u32 v = (min(wi * 48, 464) + (lane >> 4)) * (6 * D * 2) + (lane & 8) * 128 + (lane & 7) * 16;
#pragma nounroll
      for (int i = 0; i < 12; ++i) { dma(v, wmod + ca); wait_vm<PWIN>(); v += 4 * 6 * D * 2; }
      v = ((lane >> 4) * 8 + x) * (DH * D * 2) + min(wi * 12032, 119040) + (lane & 15) * 16;
#pragma nounroll
      for (int i = 0; i < 47; ++i) { dma(v, wo); wait_vm<PWIN>(); v += 256; }
    }
  }
  stream_kv(wave, (const char*)kc + cbase, (const char*)vc + cbase, lane * 16, lbase + L_CTL, lbase);

  if (wave == 2) {
    u32 d0, d1;
    asm volatile("s_load_dword %0, %2, 0x0\n\ts_load_dword %1, %3, 0x0\n\ts_waitcnt lgkmcnt(0)"
                 : "=&s"(d0), "=&s"(d1) : "s"((const char*)vc + cbase), "s"(side ? wo + (size_t)blk * 16 * D : wd + (size_t)blk * 7 * D) : "memory");
  }
  const int rbat = (g >> 2) & 3, rj = x * 2 + (g >> 4);
  const bool rr = (g & 3) == 1;
  auto until = [&](u32 t) INL { while ((u32)__builtin_amdgcn_s_memrealtime() - tstart < t) __builtin_amdgcn_s_sleep(2); };
#define TAGS() lwait(C_TAG + 2, 1, true); const u32 tag0 = (u32)ctl[C_TAG], tag1 = (u32)ctl[C_TAG + 1]; \
  const u32x4 t16 = {tag0 & 0xffffu, tag0 >> 16, tag1 & 0xffffu, tag1 >> 16}

  if (wave < 4) {
    const int ci = wave - 1, r = lane >> 4, c = lane & 15;
    if (ci == 0) {
      until(CTIME);
      const u32x2 a = ld8(rs, WS_PC + c * 8);
      const bool ok = a[1] == (u32)ctl[C_TAG];
      const float lag = row_sum(ok ? (float)(int)(tstart - a[0]) : 0.f), cnt = row_sum(ok ? 1.f : 0.f);
      ctl[C_SCH] = (int)tstart + SB0 - (int)(lag * __builtin_amdgcn_rcpf(fmaxf(cnt, 1.f)));
    }
    wait_vm<0>();
    int seen = 0, u = ci, spins = LCAP;
    bool nap = true;
    float mx = -INFINITY;
#define FETCH(d) do { for (; seen <= u && spins > 0; --spins) { seen = __builtin_amdgcn_readfirstlane(ctl[C_LANDED]); if (nap && seen <= u) __builtin_amdgcn_s_sleep(NAP); } CBAR(); \
      const int slot_ = u & (NS - 1); \
      _Pragma("unroll") for (int i_ = 0; i_ < 8; ++i_) d[i_] = *(LAS u32x4*)(L + slot_ * UNIT + i_ * 1024 + lane * 16); \
      CBAR(); ctl[C_DONE + ci] = u + 3; CBAR(); } while (0)
    auto pace = [&](int uu) INL {
      if ((uu & 15) != 0) return;
      const int k = uu >> 4;
      const u32 tg = (u32)ctl[C_TAG];
      if ((k & 1) == 0) {
        if ((g & 15) == 13 && lane == 0) st8(rs, WS_PC + k * 4352 + ((g >> 4) * 8 + x) * 8, u32x2{(u32)ctl[C_CPT + k], tg});
        return;
      }
      const u32x2 a = ld8(rs, WS_PC + (k - 1) * 4352 + c * 8);
      const bool ok = a[1] == tg;
      const float lag = row_sum(ok ? (float)(int)((u32)ctl[C_CPT + k - 1] - a[0]) : 0.f), cnt = row_sum(ok ? 1.f : 0.f);
      const int mean = ctl[C_CPT + k - 1] - (int)(lag * __builtin_amdgcn_rcpf(fmaxf(cnt, 1.f))), m2 = ctl[C_MEAN + k - 3];
      ctl[C_MEAN + k - 1] = mean;
      const int e1 = k > 3 ? (mean - m2) >> 1 : SLOPE, e2 = k > 5 ? (m2 - ctl[C_MEAN + k - 5]) >> 1 : SLOPE;
      const int slope = max(min(e1, e2), min(max(e1, e2), SLOPE));
      *(volatile LAS u64*)(L + L_CTL + C_SCH * 4) = ((u64)(u32)slope << 32) | (u32)(mean - (k - 1) * slope);
    };
    u32x4 qv = {0u, 0u, 0u, 0u};
    auto qk = [&](const u32x4 (&d)[8], int uu) INL {
      float s[8];
#pragma unroll
      for (int i = 0; i < 8; ++i) s[i] = dot2(d[i][3], qv[3], dot2(d[i][2], qv[2], dot2(d[i][1], qv[1], dot2(d[i][0], qv[0], 0.f))));
#define TR(HALF, CTRL, BIT) _Pragma("unroll") for (int h = 0; h < (HALF); ++h) { const bool up_ = (c & (BIT)) != 0; \
        const float keep_ = up_ ? s[h + (HALF)] : s[h], send_ = up_ ? s[h] : s[h + (HALF)]; s[h] = keep_ + dpp<CTRL>(send_); }
      TR(4, XOR8, 8) TR(2, XOR7, 4) TR(1, XOR2, 2)
#undef TR
      const float t = s[0] + dpp<XOR1>(s[0]);
      const u32 sb = bfbits(rb(t) * 0.08838834764831845f);
      mx = fmaxf(mx, lo2f(sb));
      *(LAS u16*)(L + L_SC + (uu * 32 + r * 8 + (c >> 1)) * 2) = (u16)sb;
    };
    u32x4 pre[NPRE - 1][8], d[8];
#pragma unroll
    for (int i = 0; i < NPRE - 1; ++i) { FETCH(pre[i]); u += 3; }
    FETCH(d);
    lwait(C_QRDY, 1, true);
    qv = *(LAS u32x4*)(L + L_Q + c * 16);
    for (int np = NPRE - 1, uu = u;;) {
      qk(d, uu);
      if (uu == u) { pace(u); u += 3; }
      if (np > 0 && seen <= u) seen = __builtin_amdgcn_readfirstlane(ctl[C_LANDED]);
      if (np > 0 && (u >= NU || seen <= u)) {
        --np; uu = ci + 3 * np;
#pragma unroll
        for (int i = 0; i < NPRE - 1; ++i) if (np == i) {
#pragma unroll
          for (int j = 0; j < 8; ++j) { d[j] = pre[i][j]; asm volatile("" : "+v"(d[j])); }
        }
      } else if (u < NU) { FETCH(d); uu = u; }
      else break;
    }
    mx = wave_max(mx);
    ctf[C_KMAX + ci] = mx;
    larrive(C_KCNT);
    lwait(C_PCNT, 4, true);
    float acc[8], sc = 0.f;
    u32 ao = L_OP + ci * 320 + (c * 8 + r * 2) * 2;
#pragma unroll
    for (int k = 0; k < 8; ++k) acc[k] = 0.f;
    auto pv = [&](u32x4 a, u32x4 b, u32 p) INL {
#pragma unroll
      for (int k = 0; k < 4; ++k) {
        acc[2 * k] = dot2(__builtin_amdgcn_perm(b[k], a[k], 0x05040100u), p, acc[2 * k]);
        acc[2 * k + 1] = dot2(__builtin_amdgcn_perm(b[k], a[k], 0x07060302u), p, acc[2 * k + 1]);
      }
    };
    for (; u < 2 * NU - 1; u += 3) {
      if (u >= 2 * NU - 4) { lwait(C_INIT, 1, true); sc = ctf[C_SCALE]; nap = false; asm volatile("" : "+v"(ao)); }
      const u32x4 pw = *(LAS u32x4*)(L + L_SC + ((u - NU) * 32 + r * 8) * 2);
      FETCH(d);
#pragma unroll
      for (int t = 0; t < 4; ++t) pv(d[2 * t], d[2 * t + 1], pw[t]);
      pace(u);
    }
    if (ci < 2) {
      u32x2 pw = *(LAS u32x2*)(L + L_SC + ((NU - 1) * 32 + r * 8) * 2 + ci * 8);
      u32 al = (NS - 1) * UNIT + ci * 4096 + lane * 16;
      asm volatile("" : "+v"(pw), "+v"(al));
      for (; seen < 2 * NU && spins > 0; --spins) seen = __builtin_amdgcn_readfirstlane(ctl[C_LANDED]);
      CBAR();
#pragma unroll
      for (int i = 0; i < 4; ++i) d[i] = *(LAS u32x4*)(L + al + i * 1024);
      pv(d[0], d[1], pw[0]); pv(d[2], d[3], pw[1]);
    }
#undef FETCH
    *(LAS u32*)(L + ao) = pk2(rowsum4(acc[0], acc[2], acc[4], acc[6]) * sc, rowsum4(acc[1], acc[3], acc[5], acc[7]) * sc);
    wait_lgkm();
    __builtin_amdgcn_s_barrier();
    if (rr && ci == 1) {
      TAGS();
      const u32 xw = *(const GAS u32*)(xin + rbat * D + rj * 32 + c * 2);
      u32x4 gm[1]; const u32 og[1] = {WS_MOD + (rbat * 4 * D + (lane & 8) * 192 + rj * 32) * 4 + (lane & 7) * 16};
      poll16(rs, gm, og, t16);
      *(LAS u32x4*)(L + L_NV + c * 16) = gm[0];
      *(LAS f32x2*)(L + L_NV + 256 + c * 8) = f32x2{lo2f(xw), hi2f(xw)};
      wait_lgkm();
    }
    asm volatile(".Lkend:");
    return;
  }

  const int w = wave - 4, kg = lane >> 4, c16 = lane & 15;
  int ts = 0;
  auto tsync = [&]() INL { larrive(C_TS); ts += 4; lwait(C_TS, ts, false); };
  u32x4 W[16], B[4][8], t0[8], mq = {0u, 0u, 0u, 0u};
  u32 mo = WS_END;
  auto wperm = [&](int s) INL {
#pragma unroll
    for (int j = 0; j < 8; ++j)
#pragma unroll
      for (int k = 0; k < 4; ++k) B[s][j][k] = __builtin_amdgcn_perm(t0[2 * k + 1][j >> 1], t0[2 * k][j >> 1], (j & 1) ? 0x07060302u : 0x05040100u);
#pragma unroll
    for (int j = 0; j < 8; ++j) asm volatile("" : "+v"(B[s][j]));
  };
  {
    if (tile) {
      u32x4 cv[4];
      if (hp || mb) {
#pragma unroll
        for (int t = 0; t < 4; ++t) cv[t] = *(const GAS u32x4*)(cin + (lane & 3) * D + t * 128 + (c16 >> 2) * 32 + kg * 8);
        const u32x4 v = *(const GAS u32x4*)(xin + w * D + lane * 8);
#pragma unroll
        for (int t = 0; t < 4; ++t) {
#pragma unroll
          for (int k = 0; k < 4; ++k) { const float p0 = lo2f(cv[t][k]), p1 = hi2f(cv[t][k]); cv[t][k] = pk2(p0 * sigmoidf_(p0), p1 * sigmoidf_(p1)); }
          asm volatile("" : "+v"(cv[t]));
        }
        float n[8], q = 0.f;
#pragma unroll
        for (int k = 0; k < 4; ++k) { n[2 * k] = lo2f(v[k]); n[2 * k + 1] = hi2f(v[k]); }
        const float mean = wave_sum(((n[0] + n[1]) + (n[2] + n[3])) + ((n[4] + n[5]) + (n[6] + n[7]))) * (1.f / D);
#pragma unroll
        for (int k = 0; k < 8; ++k) { const float e = n[k] - mean; q = fmaf(e, e, q); }
        const float sd = rb(__builtin_sqrtf(rb(rb(wave_sum(q) * (1.f / D)) + 1e-5f))), mr = rb(mean);
        if (lane >> 2 == ca >> 5) {
#pragma unroll
          for (int k = 0; k < 8; ++k) *(LAS float*)(L + L_NV + w * 128 + (lane & 3) * 32 + k * 4) = rb(rb(n[k] - mr) / sd);
        }
      }
      TAGS();
      const int fa = NS / 2 * UNIT + (kg * 8 + (c16 >> 2)) * 128 + (c16 & 2) * 32 + w * 16 + (c16 & 1) * 8;
      lwait(C_LANDED, 2, false);
#pragma unroll
      for (int s = 0; s < 16; ++s) {
        const u32x2 a = __builtin_bit_cast(u32x2, __builtin_amdgcn_ds_read_tr16_b64_v4i16((LAS s16x4*)(L + fa + s * 4096)));
        const u32x2 b = __builtin_bit_cast(u32x2, __builtin_amdgcn_ds_read_tr16_b64_v4i16((LAS s16x4*)(L + fa + s * 4096 + 512)));
        W[s] = u32x4{a[0], a[1], b[0], b[1]};
      }
#pragma unroll
      for (int i = 0; i < 16; ++i) asm volatile("" : "+v"(W[i]));
      tsync();
      ctl[C_GATE] = 2 * NU;
      if (qp) {
        u32x4 hv[2]; const u32 o0 = WS_HG + w * 2048 + lane * 16, oh[2] = {o0, o0 + 1024};
        until(HTIME);
        poll16(rs, hv, oh, t16);
#pragma unroll
        for (int i = 0; i < 2; ++i) *(LAS u32x2*)(L + L_QP + w * 1040 + i * 512 + lane * 8) = u32x2{(hv[i][0] >> 16) | hv[i][1], (hv[i][2] >> 16) | hv[i][3]};
        tsync();
#pragma unroll
        for (int t = 0; t < 4; ++t) cv[t] = *(LAS u32x4*)(L + L_QP + (lane & 3) * 1040 + t * 256 + (c16 >> 2) * 64 + kg * 16);
      }
      if (!f1) {
        f32x4 acc[4];
#pragma unroll
        for (int q = 0; q < 4; ++q) acc[q] = mfma(cv[0], W[q], f32x4{0.f, 0.f, 0.f, 0.f});
#pragma unroll
        for (int t = 1; t < 4; ++t)
#pragma unroll
          for (int q = 0; q < 4; ++q) acc[q] = mfma(cv[t], W[4 * t + q], acc[q]);
        const f32x4 v = kg == 0 ? acc[0] : kg == 1 ? acc[1] : kg == 2 ? acc[2] : acc[3];
        float mv = rb(rowsum4(v[0], v[1], v[2], v[3]));
        const float cs = dpp<XOR8>(mv);
        if (hp) mv = rb(rb(*(LAS float*)(L + L_NV + kg * 128 + (w * 8 + (c16 & 7)) * 4) * rb(1.f + cs)) + mv);
        *(LAS float*)(L + L_PART + kg * 256 + ((c16 >> 3) * 32 + w * 8 + (c16 & 7)) * 4) = mv;
        tsync();
        mq = *(LAS u32x4*)(L + L_PART + w * 256 + c16 * 16) | t16;
        mo = lane >= (hp ? 8 : 16) ? WS_END
             : (hp ? WS_HG + (w * D + ca) * 4 : qp ? WS_Q + (head * 4 + w) * 512 + (g & 4) * 64 : WS_MOD + (w * 4 * D + ca - 2 * D) * 4) + c16 * 16;
        if (!mb) st16(rs, mo, mq);
      }
    }
  }
  if (blk == 208 && w == 1) {
    u32 best[2] = {~0u, ~0u};
#pragma nounroll
    for (int i = 0; i < 16; ++i)
#pragma unroll
      for (int k = 0; k < 2; ++k) {
        u64 c0, c1; u32 v;
        asm volatile("s_memtime %0\n\ts_waitcnt lgkmcnt(0)\n\ts_load_dword %2, %3, 0x0 glc\n\ts_waitcnt lgkmcnt(0)\n\ts_memtime %1\n\ts_waitcnt lgkmcnt(0)"
                     : "=&s"(c0), "=&s"(c1), "=&s"(v) : "s"(ws + WS_PROBE + k * 8192 + (i & 3) * 2304) : "memory");
        best[k] = min(best[k], (u32)(c1 - c0));
      }
    TAGS();
    if (lane == 0) st16(rs, WS_NEAR, u32x4{best[1] < best[0] ? 0x10000u : 0u, 0u, 0u, 0u} | t16);
  }
  if (w == 3) {
    TAGS();
    u32x4 v[1]; const u32 o1[1] = {WS_Q + (head * 4 + bat) * 512 + (lane & 31) * 16};
    until(QTIME);
    poll16(rs, v, o1, t16);
    *(LAS u32x2*)(L + L_Q + (lane & 31) * 8) = u32x2{(v[0][0] >> 16) | v[0][1], (v[0][2] >> 16) | v[0][3]};
    wait_lgkm();
    ctl[C_QRDY] = 1;
  }
  until(OTIME);
  {
    const u16* p = wo + (size_t)(head * DH + kg * 8) * D + w * 128 + c16 * 8;
    for (int i = 0; i < w; ++i) __builtin_amdgcn_s_sleep(SLP_WO);
#pragma unroll
    for (int s = 0; s < 4; ++s) {
#pragma unroll
      for (int j = 0; j < 8; ++j) { CBAR(); t0[j] = *(const GAS u32x4*)(p + (size_t)(s * 32 + j) * D); CBAR(); __builtin_amdgcn_s_sleep(4 * SLP_WO); }
      wperm(s);
    }
  }
  TAGS();
  if (DOZE) doze(NU - 6);
  lwait(C_KCNT, 3, true);
  const float m = fmaxf(fmaxf(ctf[C_KMAX], ctf[C_KMAX + 1]), ctf[C_KMAX + 2]);
  {
    float l = 0.f;
#pragma unroll
    for (int i = 0; i < 2; ++i) {
      LAS u32x4* p = (LAS u32x4*)(L + L_SC + (i * 256 + w * 64 + lane) * 16);
      u32x4 v = *p;
#pragma unroll
      for (int k = 0; k < 4; ++k) {
        const float a = __builtin_amdgcn_exp2f((lo2f(v[k]) - m) * 1.4426950408889634f), b = __builtin_amdgcn_exp2f((hi2f(v[k]) - m) * 1.4426950408889634f);
        l += a + b; v[k] = pk2(a, b);
      }
      *p = v;
    }
    ctf[C_L + w] = wave_sum(l);
    larrive(C_PCNT);
  }
  if (w == 0) {
    lwait(C_PCNT, 4, false);
    const float l = (ctf[C_L] + ctf[C_L + 1]) + (ctf[C_L + 2] + ctf[C_L + 3]);
    if (lane == 0) st16(rs, WS_ML + (head * 8 + (g & 7)) * 16,
                        u32x4{__float_as_uint(m), __float_as_uint(l) & 0xffff0000u, __float_as_uint(l) << 16, 0u} | t16);
    u32x4 o;
    for (int n = GCAP >> 5; n > 0; --n) {
      __builtin_amdgcn_s_sleep(127);
      CBAR();
      o = ld16(rs, lane < 32 ? WS_ML + (head * 8 + ((g & 7) ^ 1)) * 16 : WS_NEAR) ^ t16;
      if (__builtin_amdgcn_ballot_w64((or4(o) & 0xffffu) != 0u) == 0) break;
    }
    const float m2 = __uint_as_float(o[0]), l2 = __uint_as_float(o[1] | (o[2] >> 16)), M = fmaxf(m, m2);
    const float e1 = __builtin_amdgcn_exp2f((m - M) * 1.4426950408889634f), e2 = __builtin_amdgcn_exp2f((m2 - M) * 1.4426950408889634f);
    if (lane == 0) ctf[C_SCALE] = e1 / (l * e1 + l2 * e2);
    if (lane == 32) ctl[C_NEAR] = (int)(o[0] >> 16) & 1;
    ctl[C_INIT] = 1;
  }
  if (DOZE) doze(2 * NU - 8);
  lwait(C_INIT, 1, true);
  const int near = ctl[C_NEAR];
  if (mb) { lwait(C_LANDED, MODLATE, true); st16(rs, mo, mq); }
  const u32 ox = WS_X1 + ((x & 3) * 2 + (near ^ (x >> 2))) * 8192 + w * 2048;
  if (f1 || (rr && w < 2)) {
    lwait(C_LANDED, MODLATE, true);
    const u32 off = f1 ? ox + (lane & 31) * 64
                       : WS_Y + rbat * 131072 + (near ^ (x >> 2)) * 8192 + (lane >> 3) * 16384 + (lane & 7) * 1024 + (rj & 7) * 128 + w * 64;
    asm volatile("s_mov_b32 m0, %2\n\ts_nop 0\n\tglobal_load_lds_dword %0, %1 sc1" :: "v"(off), "s"(ws), "s"(lbase + L_QP) : "memory");
    if (f1) {
#pragma unroll
      for (int i = 0; i < 8; ++i) dma(lane * 16 + i * 1024, wd + (size_t)(jf * 32 + w * 8) * D);
    }
  }
  u32 aop = L_OP + min(lane & 3, 2) * 320 + kg * 16;
  u32 yo = kg < 2 ? WS_Y + bat * 131072 + (head >> 2) * 16384 + (near ^ (w >> 1)) * 8192 + ((head & 3) * 2 + side) * 1024 + ((w & 1) * 128 + c16 * 8 + kg * 4) * 4 : WS_END;
  asm volatile("s_waitcnt lgkmcnt(0)" : "+v"(aop), "+v"(yo) :: "memory");
  __builtin_amdgcn_s_barrier();
  CBAR();
  {
    const bool first = kg == 0;
    u32x4 a[4];
#pragma unroll
    for (int s = 0; s < 4; ++s) a[s] = *(LAS u32x4*)(L + aop + s * 64);
    SCHED();
    f32x4 ac[8]; float e0, e1; u32 y0[4];
#define PIN(v) asm volatile("" : "+v"(v))
    auto sum = [&](int k, int step) INL {
      if (step == 0) { e0 = first ? ac[k][0] : ac[k + 4][0]; e1 = first ? ac[k][1] : ac[k + 4][1]; PIN(e0); PIN(e1); }
      if (step == 1) { e0 += e1; e1 = first ? ac[k][2] : ac[k + 4][2]; PIN(e0); PIN(e1); }
      if (step == 2) { e0 += e1; PIN(e0); }
      if (step == 3) { y0[k] = tag12(e0, t16[k]); PIN(y0[k]); }
      SCHED();
    };
#undef PIN
#pragma unroll
    for (int i = 0; i < 32; ++i) {
      const int j = (i >> 3) + (i & 1) * 4, s = (i >> 1) & 3;
      ac[j] = mfma(a[s], B[s][j], s ? ac[j] : f32x4{0.f, 0.f, 0.f, 0.f});
      SCHED();
      if (i >= 20) sum((i - 20) >> 2, i & 3);
    }
#pragma unroll
    for (int i = 0; i < 4; ++i) sum(3, i);
    const u32x4 y = {y0[0], y0[1], y0[2], y0[3]};
    st16(rs, yo, y);
  }
  if (rr) {
    const u32x4 t12 = t16 & 0xfffu;
    const int pr2 = (lane & 7) * 2 + (lane >> 5);
    u32 o0 = WS_Y + rbat * 131072 + (near ^ (x >> 2)) * 8192 + w * 32768 + (lane >> 3) * 1024 + (rj & 7) * 128 + (lane & 7) * 16, o1 = o0 + 16384;
    u32 aw = L_PART + w * 128 + pr2 * 8, ar = L_PART + pr2 * 8, ag = L_NV + pr2 * 8;
    const u32 xo = WS_X1 + (lane >> 3) * 8192 + rbat * 2048 + rj * 128 + (lane & 7) * 16;
    GAS u32* po = (GAS u32*)(out + rbat * D + rj * 32) + pr2;
    u32x4 t1 = t12;
#pragma nounroll
    for (int pass = 0; pass < 2; ++pass) {
      asm volatile("" : "+v"(o0), "+v"(o1), "+v"(t1), "+v"(aw), "+v"(ar), "+v"(ag));
      u32x4 v0, v1;
      for (int n = GCAP;;) {
        CBAR();
        v0 = ld16(rs, o0); v1 = ld16(rs, o1);
        u32 msk = --n > 0 ? 0xfffu : 0u;
        asm volatile("" : "+s"(msk));
        v0 ^= t12; v1 ^= t1;
        if (__builtin_amdgcn_ballot_w64(((or4(v0) | or4(v1)) & msk) != 0u) == 0) break;
      }
      const f32x4 s4 = __builtin_bit_cast(f32x4, v0) + __builtin_bit_cast(f32x4, v1);
      float a0 = tsum32(s4[0], s4[2]), a1 = tsum32(s4[1], s4[3]);
      a0 = tsum16(a0, a0); a1 = tsum16(a1, a1);
      *(LAS f32x2*)(L + aw) = f32x2{a0 + dpp<XOR8>(a0), a1 + dpp<XOR8>(a1)};
      asm volatile("s_waitcnt lgkmcnt(0)\n\ts_barrier" ::: "memory");
      if (w == 0) {
        const f32x2 sum = (*(LAS f32x2*)(L + ar) + *(LAS f32x2*)(L + ar + 128)) + (*(LAS f32x2*)(L + ar + 256) + *(LAS f32x2*)(L + ar + 384));
        const f32x2 g2 = *(LAS f32x2*)(L + ag), b2 = *(LAS f32x2*)(L + L_NV + 256 + pr2 * 8);
        const u32 sw = pk2(sum[0], sum[1]), tw = pk2(g2[0] * lo2f(sw), g2[1] * hi2f(sw)), bw = pk2(b2[0] + lo2f(tw), b2[1] + hi2f(tw));
        if (pass) { if ((lane & 24) == 0) *po = bw; break; }
        const auto pr = __builtin_amdgcn_permlane32_swap(bw, bw, false, false);
        st16(rs, xo, u32x4{pr[0] << 16, pr[0] & 0xffff0000u, pr[1] << 16, pr[1] & 0xffff0000u} | t16);
        *(LAS f32x2*)(L + L_NV + 256 + pr2 * 8) = f32x2{lo2f(bw), hi2f(bw)};
      }
      if (pass) break;
      __builtin_amdgcn_s_sleep(ZDLY);
      o0 += WS_Z - WS_Y; o1 += WS_Z - WS_Y; aw += 512; ar += 512; ag += 128;
      if (w == 3) { o1 = WS_END; t1 = u32x4{0u, 0u, 0u, 0u}; }
    }
  } else if (blk == 208 && w == 0) {
    u32x4 t[1]; const u32 o1[1] = {ox + kg * 2048};
    poll16(rs, t, o1, t16);
    if (lane == 0) st8(rs, WS_E, u32x2{tag0, tag1});
  } else if (f1) {
    {
      const u16* p = wd + (size_t)(jf * 32 + kg * 8) * D + w * 128 + c16 * 8;
#pragma unroll
      for (int j = 0; j < 8; ++j) t0[j] = *(const GAS u32x4*)(p + (size_t)j * D);
    }
    f32x2 cb1[4], sb[4];
    {
      u32x4 mv[4]; const u32 o0 = WS_MOD + (w * 4 * D + D) * 4 + lane * 16, om[4] = {o0, o0 + 1024, o0 + 2048, o0 + 3072};
      poll16(rs, mv, om, t16);
      wperm(0);
#pragma unroll
      for (int k = 0; k < 4; ++k) {
        sb[k] = f32x2{__uint_as_float(mv[k >> 1][(k & 1) * 2]), __uint_as_float(mv[k >> 1][(k & 1) * 2 + 1])};
        cb1[k] = f32x2{rb(1.f + __uint_as_float(mv[2 + (k >> 1)][(k & 1) * 2])), rb(1.f + __uint_as_float(mv[2 + (k >> 1)][(k & 1) * 2 + 1]))};
        asm volatile("" : "+v"(cb1[k]), "+v"(sb[k]));
      }
    }
    u32 ahw = L_STG + w * 1088 + lane * 8, ahr = L_STG + (c16 >> 2) * 1088 + kg * 16;
    u32 aaw = L_PART + (c16 < 8 ? kg * 64 + (w * 8 + c16) * 2 : 512 + lane * 2), aar = L_PART + ((c16 & 1) * 2 + (c16 >> 3)) * 64 + kg * 16;
    u32 zo = WS_Z + (kg >> 1) * 131072 + (jf >> 3) * 16384 + (near ^ (w >> 1)) * 8192 + (jf & 7) * 1024 + ((w & 1) * 128 + c16 * 8 + (kg & 1) * 4) * 4, zo1 = zo + 2 * 131072;
    asm volatile("" : "+v"(ahw), "+v"(ahr), "+v"(aaw), "+v"(aar), "+v"(zo), "+v"(zo1));
    u32x4 v[2]; const u32 o2[2] = {ox + lane * 16, ox + lane * 16 + 1024};
    poll16(rs, v, o2, t16);
    f32x2 e[4];
#pragma unroll
    for (int k = 0; k < 4; ++k) e[k] = f32x2{__uint_as_float(v[k >> 1][(k & 1) * 2]), __uint_as_float(v[k >> 1][(k & 1) * 2 + 1])};
    const float e0 = __builtin_bit_cast(float, __builtin_amdgcn_readfirstlane(__builtin_bit_cast(int, e[0][0])));
    const f32x2 d0 = e[0] - e0, d1 = e[1] - e0, d2 = e[2] - e0, d3 = e[3] - e0;
    const f32x2 s1 = (d0 + d1) + (d2 + d3), s2 = __builtin_elementwise_fma(d1, d1, d0 * d0) + __builtin_elementwise_fma(d3, d3, d2 * d2);
    float st = row_sum(tsum32(s1[0] + s1[1], s2[0] + s2[1]));
    st += __builtin_bit_cast(float, __builtin_amdgcn_update_dpp(0, __builtin_bit_cast(int, st), 0x142, 0xa, 0xf, false));
    st *= 1.f / D;
    const float md = __builtin_bit_cast(float, __builtin_amdgcn_readlane(__builtin_bit_cast(int, st), 31));
    const float msq = __builtin_bit_cast(float, __builtin_amdgcn_readlane(__builtin_bit_cast(int, st), 63));
    SCHED();
    u32 vm = pk2(fmaxf(fmaf(-md, md, msq), 0.f), e0 + md);
    asm volatile("" : "+v"(vm));
    SCHED();
    const float ve = rb(lo2f(vm) + 1e-5f), mr = hi2f(vm);
#pragma unroll
    for (int k = 0; k < 4; ++k) e[k] -= mr;
    SCHED();
    const float sd = rb(__builtin_amdgcn_sqrtf(ve));
#pragma unroll
    for (int k = 0; k < 4; ++k) e[k] *= cb1[k];
    SCHED();
    const float inv = __builtin_amdgcn_rcpf(sd);
#pragma unroll
    for (int k = 0; k < 4; ++k) e[k] = __builtin_elementwise_fma(e[k], f32x2{inv, inv}, sb[k]);
    SCHED();
    *(LAS u32x2*)(L + ahw) = u32x2{pk2(e[0][0], e[0][1]), pk2(e[1][0], e[1][1])};
    *(LAS u32x2*)(L + ahw + 512) = u32x2{pk2(e[2][0], e[2][1]), pk2(e[3][0], e[3][1])};
    wait_lgkm();
    __builtin_amdgcn_s_barrier();
    CBAR();
    u32x4 ah[16];
#pragma unroll
    for (int s = 0; s < 5; ++s) ah[s] = *(LAS u32x4*)(L + ahr + s * 64);
    f32x4 ga = {0.f, 0.f, 0.f, 0.f};
#pragma unroll
    for (int s = 0; s < 16; ++s) { SCHED(); ga = mfma(ah[s], W[s], ga); SCHED(); if (s + 5 < 16) ah[s + 5] = *(LAS u32x4*)(L + ahr + (s + 5) * 64); }
    const float gv = rb(ga[0]);
    float uv = dpp<XOR8>(gv);
    asm volatile("" : "+v"(uv));
    const float ge = rb(gv * __builtin_amdgcn_rcpf(1.f + __builtin_amdgcn_exp2f(gv * fmaf(gv * gv, -0.10294323958f, -2.30220819814f))));
    *(LAS u16*)(L + aaw) = (u16)bfbits(ge * uv);
    wait_lgkm();
    __builtin_amdgcn_s_barrier();
    CBAR();
    const u32x4 a = *(LAS u32x4*)(L + aar);
    f32x4 acc[8];
#pragma unroll
    for (int j = 0; j < 8; ++j) acc[j] = mfma(a, B[0][j], f32x4{0.f, 0.f, 0.f, 0.f});
    asm volatile("" : "+v"(acc[0]), "+v"(acc[1]), "+v"(acc[2]), "+v"(acc[3]), "+v"(acc[4]), "+v"(acc[5]), "+v"(acc[6]), "+v"(acc[7]));
#pragma unroll
    for (int i = 0; i < 2; ++i) {
      u32x4 z;
#pragma unroll
      for (int k = 0; k < 4; ++k) z[k] = tag12((kg & 1) ? acc[k + 4][i] : acc[k][i], t16[k]);
      st16(rs, i ? zo1 : zo, z);
    }
  }
}

extern "C" void kernel_launch(void* const* d_in, const int* in_sizes, int n_in, void* d_out, int out_size, void* d_ws, size_t ws_size,
                              hipStream_t stream) {
  adaln_block_kernel<<<256, 512, 0, stream>>>((const u16*)d_in[0], (const u16*)d_in[1], (const u16*)d_in[2], (const u16*)d_in[3], (const u16*)d_in[4],
      (const u16*)d_in[6], (const u16*)d_in[7], (const u16*)d_in[8], (const u16*)d_in[9], (const u16*)d_in[10], (u16*)d_out, (unsigned char*)d_ws);
}
```
